```python
import torch

def _softmax(x, axis=-1):
    return torch.softmax(x, dim=axis)
def _gelu(x):
    return torch.nn.functional.gelu(x, approximate="tanh")
def _silu(x):
    return torch.nn.functional.silu(x)
def _sigmoid(x):
    return torch.sigmoid(x)
def _layernorm(x, axis=-1, eps=1e-5):
    m = x.mean(dim=axis, keepdim=True)
    v = x.var(dim=axis, keepdim=True, unbiased=False)
    return (x - m) / torch.sqrt(v + eps)
def _scatter_add(tgt, idx, src, axis=0):
    return tgt.clone().index_add_(axis, idx, src)
def _segment_sum(src, seg, n):
    out = torch.zeros((n,) + src.shape[1:], dtype=src.dtype)
    return out.index_add_(0, seg, src)
def _pad(x, pw):
    flat = tuple(v for pair in reversed(pw) for v in pair)
    return torch.nn.functional.pad(x, flat)

kernel_name = 'recurrent_scan__ssm_conv_s5__glu_gated_reglu__prompt'

def setup_inputs():

    _w = (lambda a: (lambda t: t.to(torch.bfloat16) if t.is_floating_point() else t)(torch.as_tensor(a)))
    def _A_setup():
        B, S, D = 1, 2048, 448
        P = D
        Lambda_re = -0.5 * (lambda _x: torch.exp(torch.as_tensor(_x)))((lambda *s: torch.randn(*s, dtype=torch.bfloat16) * 0.8)(P))
        Lambda_im = (lambda *s: torch.randn(*s, dtype=torch.bfloat16) * 0.8)(P) * 1.0
        B_re = (lambda *s: torch.randn(*s, dtype=torch.bfloat16) * 0.8)(P, D) * (D ** -0.5)
        B_im = (lambda *s: torch.randn(*s, dtype=torch.bfloat16) * 0.8)(P, D) * (D ** -0.5)
        C_re = (lambda *s: torch.randn(*s, dtype=torch.bfloat16) * 0.8)(D, P) * (P ** -0.5)
        C_im = (lambda *s: torch.randn(*s, dtype=torch.bfloat16) * 0.8)(D, P) * (P ** -0.5)
        log_dt = (lambda *s: torch.randn(*s, dtype=torch.bfloat16) * 0.8)(P) * 0.3 - 2.0
        Dskip = (lambda *s: torch.randn(*s, dtype=torch.bfloat16) * 0.8)(D)
        return {
            "x": _w((lambda *s: torch.randn(*s, dtype=torch.bfloat16) * 0.05)(B, S, D)),
            "Lambda_re": _w(Lambda_re),
            "Lambda_im": _w(Lambda_im),
            "B_re": _w(B_re),
            "B_im": _w(B_im),
            "C_re": _w(C_re),
            "C_im": _w(C_im),
            "log_dt": _w(log_dt),
            "Dskip": _w(Dskip),
        }
    _A_in = _A_setup()

    def _B_setup():
        B, S, D, F = 1, 2048, 448, 1792
        return {
            "x": _w((lambda *s: torch.randn(*s, dtype=torch.bfloat16) * 0.05)(B, S, D)),
            "W_gate": _w((lambda *s: torch.randn(*s, dtype=torch.bfloat16) * 0.2)(D, F) * (2.0 / D) ** 0.5),
            "W_up": _w((lambda *s: torch.randn(*s, dtype=torch.bfloat16) * 0.2)(D, F) * (2.0 / D) ** 0.5),
            "W_down": _w((lambda *s: torch.randn(*s, dtype=torch.bfloat16) * 0.2)(F, D) * (1.0 / F) ** 0.5),
        }
    _B_in = _B_setup()

    return {
        "A_x": _A_in["x"],
        "A_Lambda_re": _A_in["Lambda_re"],
        "A_Lambda_im": _A_in["Lambda_im"],
        "A_B_re": _A_in["B_re"],
        "A_B_im": _A_in["B_im"],
        "A_C_re": _A_in["C_re"],
        "A_C_im": _A_in["C_im"],
        "A_log_dt": _A_in["log_dt"],
        "A_Dskip": _A_in["Dskip"],
        "B_x": _B_in["x"],
        "B_W_gate": _B_in["W_gate"],
        "B_W_up": _B_in["W_up"],
        "B_W_down": _B_in["W_down"],
    }

def reference(A_x, A_Lambda_re, A_Lambda_im, A_B_re, A_B_im, A_C_re, A_C_im, A_log_dt, A_Dskip, B_x, B_W_gate, B_W_up, B_W_down):
    def _A_ref(x, Lambda_re, Lambda_im, B_re, B_im, C_re, C_im, log_dt, Dskip):
        Bsz, L, H = x.shape
        Lam = Lambda_re + 1j * Lambda_im
        Bmat = B_re + 1j * B_im
        Cmat = C_re + 1j * C_im
        dt = torch.exp(log_dt)
        Lam_bar = torch.exp(dt * Lam)
        B_bar = ((Lam_bar - 1.0) / Lam)[:, None] * Bmat
        Bu = torch.einsum("bld,pd->blp", x + 0j, B_bar)
        h = torch.zeros_like(Bu[:, 0, :])
        hs = []
        for t in range(L):
            h = Lam_bar[None, :] * h + Bu[:, t, :]
            hs.append(h)
        hseq = torch.stack(hs, 1)
        y = 2.0 * torch.einsum("blp,dp->bld", hseq, Cmat).real + x * Dskip[None, None, :]
        return y

    def _B_ref(x, W_gate, W_up, W_down):
        gate = torch.einsum("bsd,df->bsf", x, W_gate)
        up = torch.einsum("bsd,df->bsf", x, W_up)
        gate = gate * (lambda x, dt: x.to(dt))(gate > 0, gate.dtype)
        h = gate * up
        return torch.einsum("bsf,fd->bsd", h, W_down)

    x = A_x
    xn = _layernorm(x, axis=-1)
    s = _A_ref(xn, A_Lambda_re, A_Lambda_im, A_B_re, A_B_im, A_C_re, A_C_im, A_log_dt, A_Dskip)
    g = _silu(_B_ref(xn, B_W_gate, B_W_up, B_W_down))
    return x + s * g
```

```cpp
#include <hip/hip_runtime.h>
#include <hip/hip_bf16.h>
#include <stdint.h>

#define ROUND_H 1
typedef unsigned short u16;
static constexpr int L = 2048, D = 448, P = 448, F = 1792;

__device__ __forceinline__ float bf2f(u16 v) { return __uint_as_float(((unsigned)v) << 16); }
__device__ __forceinline__ u16 f2bf(float f) { unsigned u = __float_as_uint(f); u += 0x7FFFu + ((u >> 16) & 1u); return (u16)(u >> 16); }
__device__ __forceinline__ float rbf(float f) { return bf2f(f2bf(f)); }

__global__ void k_ln(const u16* x, u16* xn) {
    int row = blockIdx.x; int lane = threadIdx.x;
    float v[7]; float s = 0.f;
    for (int i = 0; i < 7; ++i) { v[i] = bf2f(x[row * D + lane + 64 * i]); s += v[i]; }
    for (int m = 32; m >= 1; m >>= 1) s += __shfl_xor(s, m);
    float mean = s / D; float q = 0.f;
    for (int i = 0; i < 7; ++i) { float d = v[i] - mean; q += d * d; }
    for (int m = 32; m >= 1; m >>= 1) q += __shfl_xor(q, m);
    float var = rbf(q / D); float mb = rbf(mean);
    float sd = rbf(sqrtf(rbf(var + 1e-5f)));
    for (int i = 0; i < 7; ++i) xn[row * D + lane + 64 * i] = f2bf(rbf(v[i] - mb) / sd);
}
__global__ void k_gu(const u16* xn, const u16* Wg, const u16* Wu, u16* h) {
    int f = blockIdx.x * 64 + threadIdx.x; int t = blockIdx.y;
    float g = 0.f, u = 0.f;
    for (int k = 0; k < D; ++k) { float xv = bf2f(xn[t * D + k]); g += xv * bf2f(Wg[k * F + f]); u += xv * bf2f(Wu[k * F + f]); }
    g = rbf(g); u = rbf(u); g = g > 0.f ? g : 0.f;
    h[t * F + f] = f2bf(g * u);
}
__global__ void k_bu(const u16* xn, const u16* Bre, const u16* Bim, float2* bu) {
    int p = blockIdx.x * 64 + threadIdx.x; int t = blockIdx.y;
    float a = 0.f, b = 0.f;
    for (int k = 0; k < D; ++k) { float xv = bf2f(xn[t * D + k]); a += xv * bf2f(Bre[p * D + k]); b += xv * bf2f(Bim[p * D + k]); }
    bu[t * P + p] = make_float2(a, b);
}
__global__ void k_scan(const u16* Lre, const u16* Lim, const u16* logdt, const float2* bu, float2* hs) {
    int p = blockIdx.x * 64 + threadIdx.x;
    float lr = bf2f(Lre[p]), li = bf2f(Lim[p]);
    float dt = rbf(expf(bf2f(logdt[p])));
    float ar = dt * lr, ai = dt * li;
    float e = expf(ar); float br = e * cosf(ai), bi = e * sinf(ai);
    float nr = br - 1.f, ni = bi; float den = lr * lr + li * li;
    float cr = (nr * lr + ni * li) / den, ci = (ni * lr - nr * li) / den;
    float hr = 0.f, hi = 0.f;
    for (int t = 0; t < L; ++t) {
        float2 u = bu[t * P + p];
        float ur = cr * u.x - ci * u.y, ui = cr * u.y + ci * u.x;
        float nhr = br * hr - bi * hi + ur, nhi = br * hi + bi * hr + ui;
        hr = nhr; hi = nhi;
        hs[t * P + p] = make_float2(hr, hi);
    }
}
__global__ void k_fin(const u16* x, const u16* xn, const u16* h, const u16* Wd, const float2* hs, const u16* Cre, const u16* Cim, const u16* Dk, float* out) {
    int d = blockIdx.x * 64 + threadIdx.x; int t = blockIdx.y;
    float o = 0.f;
    for (int k = 0; k < F; ++k) o += bf2f(h[t * F + k]) * bf2f(Wd[k * D + d]);
    o = rbf(o);
    float g = rbf(o / (1.f + expf(-o)));
    float y = 0.f;
    for (int p = 0; p < P; ++p) { float2 v = hs[t * P + p];
        v.x = rbf(v.x); v.y = rbf(v.y);
        y += v.x * bf2f(Cre[d * P + p]) - v.y * bf2f(Cim[d * P + p]); }
    y = 2.f * y + rbf(bf2f(xn[t * D + d]) * bf2f(Dk[d]));
    out[t * D + d] = bf2f(x[t * D + d]) + y * g;
}
extern "C" void kernel_launch(void* const* d_in, const int* in_sizes, int n_in, void* d_out, int out_size, void* d_ws, size_t ws_size, hipStream_t stream) {
    const u16* x = (const u16*)d_in[0];
    const u16 *Lre = (const u16*)d_in[1], *Lim = (const u16*)d_in[2], *Bre = (const u16*)d_in[3], *Bim = (const u16*)d_in[4], *Cre = (const u16*)d_in[5], *Cim = (const u16*)d_in[6], *ldt = (const u16*)d_in[7], *Dk = (const u16*)d_in[8];
    const u16 *Wg = (const u16*)d_in[10], *Wu = (const u16*)d_in[11], *Wd = (const u16*)d_in[12];
    char* ws = (char*)d_ws;
    u16* xn = (u16*)ws; u16* h = (u16*)(ws + (4 << 20)); float2* bu = (float2*)(ws + (16 << 20)); float2* hs = (float2*)(ws + (32 << 20));
    k_ln<<<L, 64, 0, stream>>>(x, xn);
    k_gu<<<dim3(F / 64, L), 64, 0, stream>>>(xn, Wg, Wu, h);
    k_bu<<<dim3(P / 64, L), 64, 0, stream>>>(xn, Bre, Bim, bu);
    k_scan<<<P / 64, 64, 0, stream>>>(Lre, Lim, ldt, bu, hs);
    k_fin<<<dim3(D / 64, L), 64, 0, stream>>>(x, xn, h, Wd, hs, Cre, Cim, Dk, (float*)d_out);
}
```

```cpp
#include <hip/hip_runtime.h>
#include <stdint.h>

extern "C" __global__ __launch_bounds__(256, 1) void s5_fused(const uint16_t* x, const uint16_t* Lre, const uint16_t* Lim, const uint16_t* Bre,
        const uint16_t* Bim, const uint16_t* Cre, const uint16_t* Cim, const uint16_t* ldt, const uint16_t* Dk, const uint16_t* Wg,
        const uint16_t* Wu, const uint16_t* Wd, float* out, char* ws) {
    asm volatile(
        "s_mov_b64 s[4:5], %0\n"
        "s_mov_b32 s3, %1\n"
        "v_mov_b32 v252, %2\n"
        "s_load_dwordx16 s[8:23], s[4:5], 0x0\n"
        "s_load_dwordx8 s[24:31], s[4:5], 0x40\n"
        "s_load_dwordx4 s[32:35], s[4:5], 0x60\n"
        "v_lshrrev_b32 v224, 6, v252\n"
        "v_and_b32 v1, 63, v252\n"
        "v_readfirstlane_b32 s64, v224\n"
        "s_and_b32 s95, s3, 7\n"
        "s_lshr_b32 s98, s3, 3\n"
        "s_mul_i32 s71, s98, 37\n"
        "s_lshr_b32 s71, s71, 8\n"
        "s_mul_i32 s99, s71, 7\n"
        "s_sub_u32 s69, s98, s99\n"
        "s_and_b32 s70, s95, 1\n"
        "s_lshr_b32 s95, s95, 1\n"
        "s_lshl_b32 s95, s95, 2\n"
        "s_add_u32 s67, s95, s71\n"
        "s_mul_i32 s95, s70, 7\n"
        "s_add_u32 s68, s95, s69\n"
        "s_and_b32 s65, s64, 1\n"
        "s_lshr_b32 s66, s64, 1\n"
        "s_lshl_b32 s95, s71, 1\n"
        "s_add_u32 s95, s95, s69\n"
        "s_sub_u32 s98, s95, 7\n"
        "s_cmp_ge_u32 s95, 7\n"
        "s_cselect_b32 s95, s98, s95\n"
        "s_lshl_b32 s74, s95, 7\n"
        "s_add_u32 s75, s74, 128\n"
        "s_cmp_eq_u32 s75, 896\n"
        "s_cselect_b32 s75, 0, s75\n"
        "s_add_u32 s76, s75, 128\n"
        "s_cmp_eq_u32 s76, 896\n"
        "s_cselect_b32 s76, 0, s76\n"
        "s_add_u32 s77, s76, 128\n"
        "s_cmp_eq_u32 s77, 896\n"
        "s_cselect_b32 s77, 0, s77\n"
        "s_add_u32 s78, s77, 128\n"
        "s_cmp_eq_u32 s78, 896\n"
        "s_cselect_b32 s78, 0, s78\n"
        "s_add_u32 s79, s78, 128\n"
        "s_cmp_eq_u32 s79, 896\n"
        "s_cselect_b32 s79, 0, s79\n"
        "s_add_u32 s80, s79, 128\n"
        "s_cmp_eq_u32 s80, 896\n"
        "s_cselect_b32 s80, 0, s80\n"
        "v_lshrrev_b32 v225, 3, v1\n"
        "v_and_b32 v226, 7, v1\n"
        "v_lshrrev_b32 v227, 4, v1\n"
        "v_and_b32 v238, 3, v225\n"
        "v_lshrrev_b32 v239, 2, v225\n"
        "v_lshl_add_u32 v238, v239, 4, v238\n"
        "v_mul_u32_u24 v238, 896, v238\n"
        "v_xor_b32 v240, v226, v227\n"
        "v_lshlrev_b32 v240, 4, v240\n"
        "v_xor_b32 v241, 64, v240\n"
        "v_add_u32 v16, v238, v240\n"
        "v_add_u32 v17, v238, v241\n"
        "v_add_u32 v18, 5120, v16\n"
        "v_add_u32 v19, 7680, v17\n"
        "v_add_u32 v17, 2560, v17\n"
        "s_lshl_b32 s95, s67, 2\n"
        "s_add_u32 s95, s95, s64\n"
        "s_mul_i32 s1, s95, 28672\n"
        "s_mul_i32 s2, s64, 28672\n"
        "v_and_b32 v228, 31, v1\n"
        "v_lshrrev_b32 v229, 5, v1\n"
        "s_lshl_b32 s95, s68, 5\n"
        "v_add_u32 v238, s95, v228\n"
        "v_lshlrev_b32 v238, 1, v238\n"
        "s_lshl_b32 s95, s69, 1\n"
        "s_add_u32 s95, s95, s66\n"
        "s_lshl_b32 s98, s95, 5\n"
        "v_add_u32 v239, s98, v228\n"
        "v_lshlrev_b32 v239, 1, v239\n"
        "s_waitcnt lgkmcnt(0)\n"
        "s_mov_b32 s36, s8\n"
        "s_and_b32 s37, s9, 0xffff\n"
        "s_mov_b32 s38, 1835008\n"
        "s_mov_b32 s39, 0x20000\n"
        "s_add_u32 m0, s2, 0\n"
        "s_add_u32 s0, s1, s74\n"
        "buffer_load_dwordx4 v16, s[36:39], s0 offen lds\n"
        "buffer_load_dwordx4 v17, s[36:39], s0 offen offset:1024 lds\n"
        "buffer_load_dwordx4 v18, s[36:39], s0 offen offset:2048 lds\n"
        "buffer_load_dwordx4 v19, s[36:39], s0 offen offset:3072 lds\n"
        "s_mov_b32 s4, s10\n"
        "s_and_b32 s5, s11, 0xffff\n"
        "s_mov_b32 s6, 896\n"
        "s_mov_b32 s7, 0x20000\n"
        "buffer_load_ushort v234, v238, s[4:7], 0 offen\n"
        "s_mov_b32 s4, s12\n"
        "s_and_b32 s5, s13, 0xffff\n"
        "s_mov_b32 s6, 896\n"
        "s_mov_b32 s7, 0x20000\n"
        "buffer_load_ushort v235, v238, s[4:7], 0 offen\n"
        "s_mov_b32 s4, s22\n"
        "s_and_b32 s5, s23, 0xffff\n"
        "s_mov_b32 s6, 896\n"
        "s_mov_b32 s7, 0x20000\n"
        "buffer_load_ushort v236, v238, s[4:7], 0 offen\n"
        "s_mov_b32 s4, s24\n"
        "s_and_b32 s5, s25, 0xffff\n"
        "s_mov_b32 s6, 896\n"
        "s_mov_b32 s7, 0x20000\n"
        "buffer_load_ushort v237, v239, s[4:7], 0 offen\n"
        "s_mov_b32 s56, s34\n"
        "s_and_b32 s57, s35, 0xffff\n"
        "s_mov_b32 s58, 41943040\n"
        "s_mov_b32 s59, 0x20000\n"
        "v_mov_b32 v240, 0\n"
        "buffer_load_dword v242, v240, s[56:59], 0 offen sc1\n"
        "s_add_u32 m0, s2, 4096\n"
        "s_add_u32 s0, s1, s75\n"
        "buffer_load_dwordx4 v16, s[36:39], s0 offen lds\n"
        "s_cmp_eq_u32 s66, 0\n"
        "s_cselect_b32 s40, s14, s16\n"
        "s_cselect_b32 s41, s15, s17\n"
        "s_and_b32 s41, s41, 0xffff\n"
        "s_mov_b32 s42, 401408\n"
        "s_mov_b32 s43, 0x20000\n"
        "s_cmp_eq_u32 s66, 0\n"
        "s_cselect_b32 s48, s26, s28\n"
        "s_cselect_b32 s49, s27, s29\n"
        "s_and_b32 s49, s49, 0xffff\n"
        "s_mov_b32 s50, 1605632\n"
        "s_mov_b32 s51, 0x20000\n"
        "s_cmp_eq_u32 s66, 0\n"
        "s_cselect_b32 s44, s18, s20\n"
        "s_cselect_b32 s45, s19, s21\n"
        "s_and_b32 s45, s45, 0xffff\n"
        "s_mov_b32 s46, 401408\n"
        "s_mov_b32 s47, 0x20000\n"
        "s_mov_b32 s52, s30\n"
        "s_and_b32 s53, s31, 0xffff\n"
        "s_mov_b32 s54, 1605632\n"
        "s_mov_b32 s55, 0x20000\n"
        "buffer_load_dwordx4 v17, s[36:39], s0 offen offset:1024 lds\n"
        "s_mov_b32 s60, s32\n"
        "s_and_b32 s61, s33, 0xffff\n"
        "s_mov_b32 s62, 3670016\n"
        "s_mov_b32 s63, 0x20000\n"
        "v_mul_u32_u24 v238, 896, v225\n"
        "v_xor_b32 v240, v226, v227\n"
        "v_lshlrev_b32 v240, 4, v240\n"
        "v_xor_b32 v241, 64, v240\n"
        "v_add_u32 v12, v238, v240\n"
        "v_add_u32 v241, v238, v241\n"
        "v_accvgpr_write_b32 a88, v12\n"
        "v_accvgpr_write_b32 a89, v241\n"
        "v_add_u32 v13, 6144, v241\n"
        "v_lshrrev_b32 v240, 1, v225\n"
        "v_and_b32 v240, 1, v240\n"
        "v_lshlrev_b32 v240, 2, v240\n"
        "v_xor_b32 v240, v226, v240\n"
        "v_lshlrev_b32 v240, 4, v240\n"
        "v_add_u32 v241, v238, v240\n"
        "v_accvgpr_write_b32 a90, v241\n"
        "v_mul_u32_u24 v238, 3584, v225\n"
        "v_add_u32 v8, v238, v240\n"
        "buffer_load_dwordx4 v18, s[36:39], s0 offen offset:2048 lds\n"
        "v_add_u32 v9, 27648, v8\n"
        "v_add_u32 v10, 55296, v8\n"
        "v_add_u32 v11, 82944, v8\n"
        "s_lshl_b32 s95, s68, 1\n"
        "s_add_u32 s95, s95, s65\n"
        "s_mul_i32 s95, s95, 14336\n"
        "s_mul_i32 s98, s65, 114688\n"
        "s_lshl_b32 s99, s68, 8\n"
        "s_add_u32 s98, s98, s99\n"
        "s_add_u32 s81, s95, s74\n"
        "s_mul_i32 s88, s74, 1792\n"
        "s_add_u32 s88, s88, s98\n"
        "s_add_u32 s82, s95, s75\n"
        "s_mul_i32 s89, s75, 1792\n"
        "s_add_u32 s89, s89, s98\n"
        "s_add_u32 s83, s95, s76\n"
        "s_mul_i32 s90, s76, 1792\n"
        "s_add_u32 s90, s90, s98\n"
        "s_add_u32 s84, s95, s77\n"
        "s_mul_i32 s91, s77, 1792\n"
        "s_add_u32 s91, s91, s98\n"
        "s_add_u32 s85, s95, s78\n"
        "buffer_load_dwordx4 v19, s[36:39], s0 offen offset:3072 lds\n"
        "s_mul_i32 s92, s78, 1792\n"
        "s_add_u32 s92, s92, s98\n"
        "s_add_u32 s86, s95, s79\n"
        "s_mul_i32 s93, s79, 1792\n"
        "s_add_u32 s93, s93, s98\n"
        "s_add_u32 s87, s95, s80\n"
        "s_mul_i32 s94, s80, 1792\n"
        "s_add_u32 s94, s94, s98\n"
        "s_lshl_b32 s72, s64, 12\n"
        "s_lshl_b32 s73, s64, 11\n"
        "v_lshrrev_b32 v230, 1, v228\n"
        "v_and_b32 v230, 7, v230\n"
        "v_and_b32 v231, 1, v227\n"
        "v_lshrrev_b32 v232, 2, v1\n"
        "v_and_b32 v232, 3, v232\n"
        "v_and_b32 v233, 3, v1\n"
        "s_mul_i32 s95, s64, 28672\n"
        "v_lshlrev_b32 v238, 7, v228\n"
        "v_add_u32 v240, 0, v229\n"
        "v_xor_b32 v240, v240, v230\n"
        "v_lshl_add_u32 v2, v240, 4, v238\n"
        "v_add_u32 v2, s95, v2\n"
        "s_add_u32 m0, s2, 8192\n"
        "s_add_u32 s0, s1, s76\n"
        "buffer_load_dwordx4 v16, s[36:39], s0 offen lds\n"
        "v_add_u32 v240, 2, v229\n"
        "v_xor_b32 v240, v240, v230\n"
        "v_lshl_add_u32 v3, v240, 4, v238\n"
        "v_add_u32 v3, s95, v3\n"
        "v_add_u32 v240, 4, v229\n"
        "v_xor_b32 v240, v240, v230\n"
        "v_lshl_add_u32 v4, v240, 4, v238\n"
        "v_add_u32 v4, s95, v4\n"
        "v_add_u32 v240, 6, v229\n"
        "v_xor_b32 v240, v240, v230\n"
        "v_lshl_add_u32 v5, v240, 4, v238\n"
        "v_add_u32 v5, s95, v5\n"
        "v_lshlrev_b32 v240, 5, v231\n"
        "v_lshl_add_u32 v240, v233, 3, v240\n"
        "v_lshrrev_b32 v241, 1, v232\n"
        "v_lshl_add_u32 v243, v229, 3, v232\n"
        "v_xor_b32 v244, 0, v241\n"
        "v_lshlrev_b32 v244, 6, v244\n"
        "v_lshl_add_u32 v6, v243, 7, v244\n"
        "v_add_u32 v6, v6, v240\n"
        "v_xor_b32 v244, 1, v241\n"
        "v_lshlrev_b32 v244, 6, v244\n"
        "buffer_load_dwordx4 v17, s[36:39], s0 offen offset:1024 lds\n"
        "v_lshl_add_u32 v7, v243, 7, v244\n"
        "v_add_u32 v7, v7, v240\n"
        "v_lshl_add_u32 v245, v229, 2, v232\n"
        "s_lshl_b32 s95, s66, 13\n"
        "s_xor_b32 s98, s66, 0\n"
        "v_xor_b32 v244, s98, v241\n"
        "v_lshlrev_b32 v244, 6, v244\n"
        "v_lshl_add_u32 v244, v245, 7, v244\n"
        "v_add_u32 v244, v244, v240\n"
        "v_add_u32 v244, s95, v244\n"
        "v_accvgpr_write_b32 a91, v244\n"
        "s_xor_b32 s98, s66, 1\n"
        "v_xor_b32 v244, s98, v241\n"
        "v_lshlrev_b32 v244, 6, v244\n"
        "v_lshl_add_u32 v244, v245, 7, v244\n"
        "v_add_u32 v244, v244, v240\n"
        "v_add_u32 v244, s95, v244\n"
        "v_accvgpr_write_b32 a92, v244\n"
        "s_mul_i32 s95, s64, 2304\n"
        "s_add_u32 s95, s95, 147456\n"
        "v_mul_u32_u24 v244, 72, v228\n"
        "v_lshl_add_u32 v244, v229, 3, v244\n"
        "buffer_load_dwordx4 v18, s[36:39], s0 offen offset:2048 lds\n"
        "v_add_u32 v244, s95, v244\n"
        "v_accvgpr_write_b32 a93, v244\n"
        "v_mul_u32_u24 v244, 72, v243\n"
        "v_add_u32 v244, v244, v240\n"
        "v_add_u32 v244, s95, v244\n"
        "v_accvgpr_write_b32 a94, v244\n"
        "v_lshlrev_b32 v246, 4, v1\n"
        "s_lshl_b32 s95, s67, 2\n"
        "s_add_u32 s95, s95, s64\n"
        "s_mul_i32 s98, s95, 14\n"
        "s_add_u32 s98, s98, s68\n"
        "s_lshl_b32 s98, s98, 13\n"
        "s_add_u32 s98, s98, 1048576\n"
        "v_add_u32 v244, s98, v246\n"
        "v_accvgpr_write_b32 a95, v244\n"
        "s_mul_i32 s98, s95, 7\n"
        "s_lshr_b32 s99, s68, 1\n"
        "s_add_u32 s98, s98, s99\n"
        "s_lshl_b32 s98, s98, 13\n"
        "s_and_b32 s99, s68, 1\n"
        "s_lshl_b32 s99, s99, 11\n"
        "s_add_u32 s98, s98, s99\n"
        "buffer_load_dwordx4 v19, s[36:39], s0 offen offset:3072 lds\n"
        "s_add_u32 s98, s98, 16777216\n"
        "v_add_u32 v244, s98, v246\n"
        "v_accvgpr_write_b32 a96, v244\n"
        "s_lshl_b32 s95, s67, 1\n"
        "s_add_u32 s95, s95, s70\n"
        "s_lshl_b32 s95, s95, 1\n"
        "s_add_u32 s95, s95, s65\n"
        "s_mul_i32 s98, s95, 14\n"
        "s_mul_i32 s99, s66, 7\n"
        "s_add_u32 s98, s98, s99\n"
        "s_lshl_b32 s98, s98, 13\n"
        "s_add_u32 s98, s98, 1048576\n"
        "s_mul_i32 s99, s95, 57344\n"
        "s_lshl_b32 s100, s66, 12\n"
        "s_add_u32 s99, s99, s100\n"
        "s_add_u32 s99, s99, 16777216\n"
        "s_mul_i32 s100, s66, 802816\n"
        "s_mul_i32 s101, s65, 28672\n"
        "s_add_u32 s100, s100, s101\n"
        "s_lshl_b32 s101, s69, 7\n"
        "s_add_u32 s100, s100, s101\n"
        "s_lshl_b32 s101, s69, 1\n"
        "s_add_u32 m0, s2, 12288\n"
        "s_add_u32 s0, s1, s77\n"
        "buffer_load_dwordx4 v16, s[36:39], s0 offen lds\n"
        "s_add_u32 s101, s101, s65\n"
        "s_mul_i32 s101, s101, 28672\n"
        "s_lshl_b32 s95, s74, 6\n"
        "s_add_u32 s15, s95, s98\n"
        "s_add_u32 s22, s95, s99\n"
        "s_mul_i32 s8, s74, 896\n"
        "s_add_u32 s8, s8, s100\n"
        "s_add_u32 s29, s74, s101\n"
        "s_lshl_b32 s95, s75, 6\n"
        "s_add_u32 s16, s95, s98\n"
        "s_add_u32 s23, s95, s99\n"
        "s_mul_i32 s9, s75, 896\n"
        "s_add_u32 s9, s9, s100\n"
        "s_add_u32 s30, s75, s101\n"
        "s_lshl_b32 s95, s76, 6\n"
        "s_add_u32 s17, s95, s98\n"
        "s_add_u32 s24, s95, s99\n"
        "s_mul_i32 s10, s76, 896\n"
        "s_add_u32 s10, s10, s100\n"
        "s_add_u32 s31, s76, s101\n"
        "s_lshl_b32 s95, s77, 6\n"
        "s_add_u32 s18, s95, s98\n"
        "buffer_load_dwordx4 v17, s[36:39], s0 offen offset:1024 lds\n"
        "s_add_u32 s25, s95, s99\n"
        "s_mul_i32 s11, s77, 896\n"
        "s_add_u32 s11, s11, s100\n"
        "s_add_u32 s32, s77, s101\n"
        "s_lshl_b32 s95, s78, 6\n"
        "s_add_u32 s19, s95, s98\n"
        "s_add_u32 s26, s95, s99\n"
        "s_mul_i32 s12, s78, 896\n"
        "s_add_u32 s12, s12, s100\n"
        "s_add_u32 s33, s78, s101\n"
        "s_lshl_b32 s95, s79, 6\n"
        "s_add_u32 s20, s95, s98\n"
        "s_add_u32 s27, s95, s99\n"
        "s_mul_i32 s13, s79, 896\n"
        "s_add_u32 s13, s13, s100\n"
        "s_add_u32 s34, s79, s101\n"
        "s_lshl_b32 s95, s80, 6\n"
        "s_add_u32 s21, s95, s98\n"
        "s_add_u32 s28, s95, s99\n"
        "s_mul_i32 s14, s80, 896\n"
        "s_add_u32 s14, s14, s100\n"
        "s_add_u32 s35, s80, s101\n"
        "buffer_load_dwordx4 v18, s[36:39], s0 offen offset:2048 lds\n"
        "s_lshl_b32 s95, s64, 1\n"
        "v_add_u32 v247, s95, v229\n"
        "v_accvgpr_write_b32 a97, v247\n"
        "v_lshlrev_b32 v244, 3, v228\n"
        "v_add_u32 v244, 157696, v244\n"
        "v_accvgpr_write_b32 a98, v244\n"
        "v_lshl_add_u32 v245, v247, 8, v244\n"
        "v_accvgpr_write_b32 a99, v245\n"
        "v_lshlrev_b32 v245, 8, v229\n"
        "v_lshl_add_u32 v245, v245, 3, v244\n"
        "v_add_u32 v245, 2048, v245\n"
        "v_accvgpr_write_b32 a100, v245\n"
        "s_lshl_b32 s95, s64, 2\n"
        "v_add_u32 v245, s95, v229\n"
        "v_lshl_add_u32 v248, v245, 8, v244\n"
        "v_add_u32 v248, 2048, v248\n"
        "v_accvgpr_write_b32 a101, v248\n"
        "s_sub_u32 s95, s67, 16\n"
        "v_add_u32 v245, s95, v245\n"
        "s_lshl_b32 s98, s68, 9\n"
        "s_add_u32 s98, s98, 131072\n"
        "v_lshlrev_b32 v248, 4, v228\n"
        "buffer_load_dwordx4 v19, s[36:39], s0 offen offset:3072 lds\n"
        "v_add_u32 v248, s98, v248\n"
        "v_mov_b32 v251, 2147479552\n"
        "v_mul_i32_i24 v249, 7168, v245\n"
        "v_add_u32 v249, v249, v248\n"
        "v_cmp_le_i32 vcc, 0, v245\n"
        "s_nop 1\n"
        "v_cndmask_b32 v249, v251, v249, vcc\n"
        "v_accvgpr_write_b32 a102, v249\n"
        "v_cndmask_b32 v250, 0, -1, vcc\n"
        "v_accvgpr_write_b32 a103, v250\n"
        "v_add_u32 v245, 2, v245\n"
        "v_mul_i32_i24 v249, 7168, v245\n"
        "v_add_u32 v249, v249, v248\n"
        "v_cmp_le_i32 vcc, 0, v245\n"
        "s_nop 1\n"
        "v_cndmask_b32 v249, v251, v249, vcc\n"
        "v_accvgpr_write_b32 a104, v249\n"
        "v_cndmask_b32 v250, 0, -1, vcc\n"
        "v_accvgpr_write_b32 a105, v250\n"
        "s_mul_i32 s95, s67, 7168\n"
        "v_add_u32 v249, s95, v248\n"
        "v_lshrrev_b32 v250, 3, v1\n"
        "v_cmp_eq_u32 vcc, s64, v250\n"
        "s_nop 1\n"
        "v_cndmask_b32 v249, v251, v249, vcc\n"
        "s_add_u32 m0, s2, 16384\n"
        "s_add_u32 s0, s1, s78\n"
        "buffer_load_dwordx4 v16, s[36:39], s0 offen lds\n"
        "v_accvgpr_write_b32 a106, v249\n"
        "s_mul_i32 s95, s67, 42\n"
        "s_add_u32 s98, s95, s68\n"
        "s_lshl_b32 s98, s98, 7\n"
        "s_add_u32 s98, s98, 4096\n"
        "v_mov_b32 v249, s98\n"
        "v_cmp_eq_u32 vcc, 0, v252\n"
        "s_nop 1\n"
        "v_cndmask_b32 v249, v251, v249, vcc\n"
        "v_accvgpr_write_b32 a107, v249\n"
        "v_min_u32 v249, 13, v1\n"
        "v_add_u32 v249, s95, v249\n"
        "v_lshlrev_b32 v249, 7, v249\n"
        "v_add_u32 v249, 4096, v249\n"
        "v_accvgpr_write_b32 a108, v249\n"
        "v_mul_u32_u24 v249, 4682, v252\n"
        "v_lshrrev_b32 v249, 16, v249\n"
        "v_mul_u32_u24 v250, 28, v249\n"
        "v_add_u32 v250, v250, v252\n"
        "v_add_u32 v250, 28, v250\n"
        "v_lshlrev_b32 v250, 7, v250\n"
        "v_add_u32 v250, 4096, v250\n"
        "s_cmp_eq_u32 s3, 0\n"
        "s_cselect_b32 s95, 224, 0\n"
        "buffer_load_dwordx4 v17, s[36:39], s0 offen offset:1024 lds\n"
        "v_cmp_gt_u32 vcc, s95, v252\n"
        "s_nop 1\n"
        "v_cndmask_b32 v250, v251, v250, vcc\n"
        "v_accvgpr_write_b32 a109, v250\n"
        "v_cndmask_b32 v250, 0, -1, vcc\n"
        "v_accvgpr_write_b32 a110, v250\n"
        "s_lshl_b32 s95, s67, 1\n"
        "s_add_u32 s95, s95, s70\n"
        "s_lshl_b32 s95, s95, 1\n"
        "s_add_u32 s95, s95, s65\n"
        "s_lshl_b32 s95, s95, 5\n"
        "v_lshl_add_u32 v249, v229, 4, s95\n"
        "v_mul_u32_u24 v249, 896, v249\n"
        "v_add_u32 v249, v249, v239\n"
        "v_accvgpr_write_b32 a111, v249\n"
        "s_lshl_b32 s95, s70, 1\n"
        "s_add_u32 s95, s95, s65\n"
        "s_lshl_b32 s95, s95, 8\n"
        "s_add_u32 s95, s95, 156672\n"
        "v_lshl_add_u32 v249, v229, 7, s95\n"
        "v_accvgpr_write_b32 a112, v249\n"
        "v_and_b32 v249, 3, v228\n"
        "v_lshrrev_b32 v250, 3, v228\n"
        "buffer_load_dwordx4 v18, s[36:39], s0 offen offset:2048 lds\n"
        "v_lshl_add_u32 v249, v250, 2, v249\n"
        "v_lshrrev_b32 v250, 2, v228\n"
        "v_and_b32 v250, 1, v250\n"
        "v_lshl_add_u32 v249, v250, 4, v249\n"
        "s_lshl_b32 s95, s64, 8\n"
        "s_add_u32 s95, s95, 156672\n"
        "v_lshl_add_u32 v249, v249, 3, s95\n"
        "v_accvgpr_write_b32 a113, v249\n"
        "s_waitcnt vmcnt(15)\n"
        "v_readfirstlane_b32 s95, v242\n"
        "s_add_u32 s96, s95, 1\n"
        "s_not_b32 s97, s96\n"
        "v_lshlrev_b32 v160, 16, v234\n"
        "v_lshlrev_b32 v161, 16, v235\n"
        "v_lshlrev_b32 v163, 16, v236\n"
        "v_lshlrev_b32 v164, 16, v237\n"
        "v_accvgpr_write_b32 a114, v164\n"
        "v_mul_f32 v164, 0x3fb8aa3b, v163\n"
        "v_mov_b32 v165, 0x3fb8aa3b\n"
        "v_fma_f32 v165, v163, v165, -v164\n"
        "v_mov_b32 v199, 0x32a57060\n"
        "v_fmac_f32 v165, v163, v199\n"
        "buffer_load_dwordx4 v19, s[36:39], s0 offen offset:3072 lds\n"
        "v_exp_f32 v162, v164\n"
        "v_mul_f32 v165, 0x3f317218, v165\n"
        "v_fma_f32 v162, v162, v165, v162\n"
        "v_cvt_pk_bf16_f32 v162, v162, v162\n"
        "v_lshlrev_b32 v162, 16, v162\n"
        "v_mul_f32 v163, v162, v160\n"
        "v_mul_f32 v166, v162, v161\n"
        "v_mul_f32 v164, 0x3fb8aa3b, v163\n"
        "v_mov_b32 v165, 0x3fb8aa3b\n"
        "v_fma_f32 v165, v163, v165, -v164\n"
        "v_mov_b32 v199, 0x32a57060\n"
        "v_fmac_f32 v165, v163, v199\n"
        "v_exp_f32 v167, v164\n"
        "v_mul_f32 v165, 0x3f317218, v165\n"
        "v_fma_f32 v167, v167, v165, v167\n"
        "v_mul_f32 v168, 0x3f22f983, v166\n"
        "v_rndne_f32 v168, v168\n"
        "v_mov_b32 v164, 0xbfc90fdb\n"
        "v_fma_f32 v169, v168, v164, v166\n"
        "v_mov_b32 v164, 0x333bbd2e\n"
        "v_fma_f32 v169, v168, v164, v169\n"
        "v_mov_b32 v164, 0x27000000\n"
        "s_add_u32 m0, s2, 20480\n"
        "s_add_u32 s0, s1, s79\n"
        "buffer_load_dwordx4 v16, s[36:39], s0 offen lds\n"
        "v_fma_f32 v169, v168, v164, v169\n"
        "v_mul_f32 v170, v169, v169\n"
        "v_mov_b32 v171, 0xb94ca1f9\n"
        "v_mov_b32 v164, 0x3c08839e\n"
        "v_fma_f32 v171, v171, v170, v164\n"
        "v_mov_b32 v164, 0xbe2aaaa3\n"
        "v_fma_f32 v171, v171, v170, v164\n"
        "v_mul_f32 v171, v171, v170\n"
        "v_fma_f32 v171, v171, v169, v169\n"
        "v_mov_b32 v172, 0x37ccf5ce\n"
        "v_mov_b32 v164, 0xbab6061a\n"
        "v_fma_f32 v172, v172, v170, v164\n"
        "v_mov_b32 v164, 0x3d2aaaa5\n"
        "v_fma_f32 v172, v172, v170, v164\n"
        "v_mul_f32 v172, v172, v170\n"
        "v_add_f32 v172, -0.5, v172\n"
        "v_fma_f32 v172, v172, v170, 1.0\n"
        "v_cvt_i32_f32 v168, v168\n"
        "v_and_b32 v164, 1, v168\n"
        "v_cmp_eq_u32 vcc, 1, v164\n"
        "s_nop 1\n"
        "v_cndmask_b32 v165, v171, v172, vcc\n"
        "v_cndmask_b32 v173, v172, v171, vcc\n"
        "buffer_load_dwordx4 v17, s[36:39], s0 offen offset:1024 lds\n"
        "v_lshlrev_b32 v164, 30, v168\n"
        "v_and_b32 v164, 0x80000000, v164\n"
        "v_xor_b32 v171, v164, v165\n"
        "v_add_u32 v164, 1, v168\n"
        "v_lshlrev_b32 v164, 30, v164\n"
        "v_and_b32 v164, 0x80000000, v164\n"
        "v_xor_b32 v172, v164, v173\n"
        "v_mul_f32 v174, v167, v172\n"
        "v_mul_f32 v175, v167, v171\n"
        "v_accvgpr_write_b32 a115, v174\n"
        "v_accvgpr_write_b32 a116, v175\n"
        "v_add_f32 v176, -1.0, v174\n"
        "v_mul_f32 v177, v160, v160\n"
        "v_fmac_f32 v177, v161, v161\n"
        "v_rcp_f32 v178, v177\n"
        "v_mul_f32 v179, v176, v160\n"
        "v_fmac_f32 v179, v175, v161\n"
        "v_mul_f32 v164, v179, v178\n"
        "v_fma_f32 v165, -v164, v177, v179\n"
        "v_fmac_f32 v164, v165, v178\n"
        "v_add_f32 v180, v164, v164\n"
        "v_accvgpr_write_b32 a117, v180\n"
        "buffer_load_dwordx4 v18, s[36:39], s0 offen offset:2048 lds\n"
        "v_mul_f32 v179, v175, v160\n"
        "v_fma_f32 v179, -v176, v161, v179\n"
        "v_mul_f32 v164, v179, v178\n"
        "v_fma_f32 v165, -v164, v177, v179\n"
        "v_fmac_f32 v164, v165, v178\n"
        "v_add_f32 v180, v164, v164\n"
        "v_accvgpr_write_b32 a118, v180\n"
        "v_mov_b32 v181, v174\n"
        "v_mov_b32 v182, v175\n"
        "v_mul_f32 v164, v182, v182\n"
        "v_mul_f32 v165, v181, v182\n"
        "v_fma_f32 v181, v181, v181, -v164\n"
        "v_add_f32 v182, v165, v165\n"
        "v_mul_f32 v164, v182, v182\n"
        "v_mul_f32 v165, v181, v182\n"
        "v_fma_f32 v181, v181, v181, -v164\n"
        "v_add_f32 v182, v165, v165\n"
        "v_mul_f32 v164, v182, v182\n"
        "v_mul_f32 v165, v181, v182\n"
        "v_fma_f32 v181, v181, v181, -v164\n"
        "v_add_f32 v182, v165, v165\n"
        "v_mul_f32 v164, v182, v182\n"
        "buffer_load_dwordx4 v19, s[36:39], s0 offen offset:3072 lds\n"
        "v_mul_f32 v165, v181, v182\n"
        "v_fma_f32 v181, v181, v181, -v164\n"
        "v_add_f32 v182, v165, v165\n"
        "v_accvgpr_write_b32 a119, v181\n"
        "v_accvgpr_write_b32 a120, v182\n"
        "v_and_b32 v164, 1, v247\n"
        "v_cmp_eq_u32 vcc, 1, v164\n"
        "v_mov_b32 v185, 1.0\n"
        "v_mov_b32 v186, 0\n"
        "v_cndmask_b32 v183, v185, v181, vcc\n"
        "v_cndmask_b32 v184, v186, v182, vcc\n"
        "v_mul_f32 v164, v182, v182\n"
        "v_mul_f32 v165, v181, v182\n"
        "v_fma_f32 v181, v181, v181, -v164\n"
        "v_add_f32 v182, v165, v165\n"
        "v_lshrrev_b32 v187, 1, v247\n"
        "v_and_b32 v187, 1, v187\n"
        "v_cmp_eq_u32 vcc, 1, v187\n"
        "s_nop 1\n"
        "v_cndmask_b32 v187, v185, v181, vcc\n"
        "v_cndmask_b32 v188, v186, v182, vcc\n"
        "v_mul_f32 v164, v184, v188\n"
        "v_mul_f32 v165, v183, v188\n"
        "s_add_u32 m0, s2, 24576\n"
        "s_add_u32 s0, s1, s80\n"
        "buffer_load_dwordx4 v16, s[36:39], s0 offen lds\n"
        "v_fma_f32 v183, v183, v187, -v164\n"
        "v_fma_f32 v184, v184, v187, v165\n"
        "v_mul_f32 v164, v182, v182\n"
        "v_mul_f32 v165, v181, v182\n"
        "v_fma_f32 v181, v181, v181, -v164\n"
        "v_add_f32 v182, v165, v165\n"
        "v_lshrrev_b32 v187, 2, v247\n"
        "v_and_b32 v187, 1, v187\n"
        "v_cmp_eq_u32 vcc, 1, v187\n"
        "s_nop 1\n"
        "v_cndmask_b32 v187, v185, v181, vcc\n"
        "v_cndmask_b32 v188, v186, v182, vcc\n"
        "v_mul_f32 v164, v184, v188\n"
        "v_mul_f32 v165, v183, v188\n"
        "v_fma_f32 v183, v183, v187, -v164\n"
        "v_fma_f32 v184, v184, v187, v165\n"
        "v_accvgpr_write_b32 a121, v183\n"
        "v_accvgpr_write_b32 a122, v184\n"
        "v_mul_f32 v164, v182, v182\n"
        "v_mul_f32 v165, v181, v182\n"
        "v_fma_f32 v181, v181, v181, -v164\n"
        "v_add_f32 v182, v165, v165\n"
        "v_accvgpr_write_b32 a123, v181\n"
        "buffer_load_dwordx4 v17, s[36:39], s0 offen offset:1024 lds\n"
        "v_accvgpr_write_b32 a124, v182\n"
        "v_mul_f32 v164, v182, v182\n"
        "v_mul_f32 v165, v181, v182\n"
        "v_fma_f32 v181, v181, v181, -v164\n"
        "v_add_f32 v182, v165, v165\n"
        "v_mul_f32 v164, v182, v182\n"
        "v_mul_f32 v165, v181, v182\n"
        "v_fma_f32 v181, v181, v181, -v164\n"
        "v_add_f32 v182, v165, v165\n"
        "v_mul_f32 v164, v182, v182\n"
        "v_mul_f32 v165, v181, v182\n"
        "v_fma_f32 v181, v181, v181, -v164\n"
        "v_add_f32 v182, v165, v165\n"
        "v_accvgpr_write_b32 a125, v181\n"
        "v_accvgpr_write_b32 a126, v182\n"
        "v_lshlrev_b32 v1, 4, v1\n"
        "buffer_load_dwordx4 v18, s[36:39], s0 offen offset:2048 lds\n"
        "buffer_load_dwordx4 v19, s[36:39], s0 offen offset:3072 lds\n"
        "v_mov_b32 v224, 0\n"
        "v_mov_b32 v225, 0\n"
        "v_mov_b32 v226, 0\n"
        "v_mov_b32 v227, 0\n"
        "v_mov_b32 v228, 0x3f803f80\n"
        "s_waitcnt vmcnt(29)\n"
        "ds_read_b128 v[16:19], v2\n"
        "ds_read_b128 v[20:23], v3\n"
        "ds_read_b128 v[24:27], v4\n"
        "ds_read_b128 v[28:31], v5\n"
        "s_waitcnt vmcnt(20)\n"
        "ds_read_b128 v[32:35], v2 offset:4096\n"
        "ds_read_b128 v[36:39], v3 offset:4096\n"
        "ds_read_b128 v[40:43], v4 offset:4096\n"
        "ds_read_b128 v[44:47], v5 offset:4096\n"
        "s_waitcnt lgkmcnt(7)\n"
        "v_dot2c_f32_bf16 v224, v16, v228\n"
        "v_dot2c_f32_bf16 v226, v16, v16\n"
        "v_dot2c_f32_bf16 v225, v17, v228\n"
        "v_dot2c_f32_bf16 v227, v17, v17\n"
        "v_dot2c_f32_bf16 v224, v18, v228\n"
        "v_dot2c_f32_bf16 v226, v18, v18\n"
        "s_add_u32 m0, s73, 114688\n"
        "s_nop 0\n"
        "buffer_load_dwordx4 v12, s[40:43], s81 offen lds\n"
        "v_dot2c_f32_bf16 v225, v19, v228\n"
        "v_dot2c_f32_bf16 v227, v19, v19\n"
        "s_waitcnt lgkmcnt(6)\n"
        "v_dot2c_f32_bf16 v224, v20, v228\n"
        "v_dot2c_f32_bf16 v226, v20, v20\n"
        "v_dot2c_f32_bf16 v225, v21, v228\n"
        "v_dot2c_f32_bf16 v227, v21, v21\n"
        "v_dot2c_f32_bf16 v224, v22, v228\n"
        "v_dot2c_f32_bf16 v226, v22, v22\n"
        "v_dot2c_f32_bf16 v225, v23, v228\n"
        "v_dot2c_f32_bf16 v227, v23, v23\n"
        "s_waitcnt lgkmcnt(5)\n"
        "buffer_load_dwordx4 v13, s[40:43], s81 offen offset:1024 lds\n"
        "v_dot2c_f32_bf16 v224, v24, v228\n"
        "v_dot2c_f32_bf16 v226, v24, v24\n"
        "v_dot2c_f32_bf16 v225, v25, v228\n"
        "v_dot2c_f32_bf16 v227, v25, v25\n"
        "v_dot2c_f32_bf16 v224, v26, v228\n"
        "v_dot2c_f32_bf16 v226, v26, v26\n"
        "v_dot2c_f32_bf16 v225, v27, v228\n"
        "v_dot2c_f32_bf16 v227, v27, v27\n"
        "s_waitcnt lgkmcnt(4)\n"
        "v_dot2c_f32_bf16 v224, v28, v228\n"
        "v_dot2c_f32_bf16 v226, v28, v28\n"
        "v_dot2c_f32_bf16 v225, v29, v228\n"
        "s_add_u32 m0, s72, 122880\n"
        "s_nop 0\n"
        "buffer_load_dwordx4 v8, s[48:51], s88 offen lds\n"
        "v_dot2c_f32_bf16 v227, v29, v29\n"
        "v_dot2c_f32_bf16 v224, v30, v228\n"
        "v_dot2c_f32_bf16 v226, v30, v30\n"
        "v_dot2c_f32_bf16 v225, v31, v228\n"
        "v_dot2c_f32_bf16 v227, v31, v31\n"
        "s_waitcnt vmcnt(19)\n"
        "ds_read_b128 v[48:51], v2 offset:8192\n"
        "ds_read_b128 v[52:55], v3 offset:8192\n"
        "ds_read_b128 v[56:59], v4 offset:8192\n"
        "ds_read_b128 v[60:63], v5 offset:8192\n"
        "s_waitcnt lgkmcnt(7)\n"
        "v_dot2c_f32_bf16 v224, v32, v228\n"
        "buffer_load_dwordx4 v9, s[48:51], s88 offen offset:1024 lds\n"
        "v_dot2c_f32_bf16 v226, v32, v32\n"
        "v_dot2c_f32_bf16 v225, v33, v228\n"
        "v_dot2c_f32_bf16 v227, v33, v33\n"
        "v_dot2c_f32_bf16 v224, v34, v228\n"
        "v_dot2c_f32_bf16 v226, v34, v34\n"
        "v_dot2c_f32_bf16 v225, v35, v228\n"
        "v_dot2c_f32_bf16 v227, v35, v35\n"
        "s_waitcnt lgkmcnt(6)\n"
        "v_dot2c_f32_bf16 v224, v36, v228\n"
        "v_dot2c_f32_bf16 v226, v36, v36\n"
        "v_dot2c_f32_bf16 v225, v37, v228\n"
        "v_dot2c_f32_bf16 v227, v37, v37\n"
        "buffer_load_dwordx4 v10, s[48:51], s88 offen offset:2048 lds\n"
        "v_dot2c_f32_bf16 v224, v38, v228\n"
        "v_dot2c_f32_bf16 v226, v38, v38\n"
        "v_dot2c_f32_bf16 v225, v39, v228\n"
        "v_dot2c_f32_bf16 v227, v39, v39\n"
        "s_waitcnt lgkmcnt(5)\n"
        "v_dot2c_f32_bf16 v224, v40, v228\n"
        "v_dot2c_f32_bf16 v226, v40, v40\n"
        "v_dot2c_f32_bf16 v225, v41, v228\n"
        "v_dot2c_f32_bf16 v227, v41, v41\n"
        "v_dot2c_f32_bf16 v224, v42, v228\n"
        "v_dot2c_f32_bf16 v226, v42, v42\n"
        "v_dot2c_f32_bf16 v225, v43, v228\n"
        "buffer_load_dwordx4 v11, s[48:51], s88 offen offset:3072 lds\n"
        "v_dot2c_f32_bf16 v227, v43, v43\n"
        "s_waitcnt lgkmcnt(4)\n"
        "v_dot2c_f32_bf16 v224, v44, v228\n"
        "v_dot2c_f32_bf16 v226, v44, v44\n"
        "v_dot2c_f32_bf16 v225, v45, v228\n"
        "v_dot2c_f32_bf16 v227, v45, v45\n"
        "v_dot2c_f32_bf16 v224, v46, v228\n"
        "v_dot2c_f32_bf16 v226, v46, v46\n"
        "v_dot2c_f32_bf16 v225, v47, v228\n"
        "v_dot2c_f32_bf16 v227, v47, v47\n"
        "s_waitcnt vmcnt(18)\n"
        "ds_read_b128 v[64:67], v2 offset:12288\n"
        "s_getpc_b64 s[4:5]\n"
        ".Lpf%=:\n"
        "s_and_b32 s4, s4, 0xffffff80\n"
        "s_and_b32 s5, s5, 0xffff\n"
        "s_mov_b32 s6, .Lend%=-.Lpf%=\n"
        "s_mov_b32 s7, 0x20000\n"
        "s_lshr_b32 s2, s3, 3\n"
        "s_mul_i32 s2, s2, 12\n"
        "v_and_b32 v128, 63, v252\n"
        "v_add_u32 v129, s2, v128\n"
        "v_lshlrev_b32 v129, 7, v129\n"
        "s_cmp_eq_u32 s64, 0\n"
        "s_cselect_b32 s2, 12, 0\n"
        "v_cmp_gt_u32 s[0:1], s2, v128\n"
        "v_mov_b32 v130, 2147479552\n"
        "s_nop 0\n"
        "v_cndmask_b32 v129, v130, v129, s[0:1]\n"
        "s_add_u32 m0, s72, 147456\n"
        "s_nop 0\n"
        "buffer_load_dword v129, s[4:7], 0 offen lds\n"
        "ds_read_b128 v[68:71], v3 offset:12288\n"
        "ds_read_b128 v[72:75], v4 offset:12288\n"
        "ds_read_b128 v[76:79], v5 offset:12288\n"
        "s_waitcnt lgkmcnt(7)\n"
        "v_dot2c_f32_bf16 v224, v48, v228\n"
        "v_dot2c_f32_bf16 v226, v48, v48\n"
        "v_dot2c_f32_bf16 v225, v49, v228\n"
        "v_dot2c_f32_bf16 v227, v49, v49\n"
        "v_dot2c_f32_bf16 v224, v50, v228\n"
        "v_dot2c_f32_bf16 v226, v50, v50\n"
        "v_dot2c_f32_bf16 v225, v51, v228\n"
        "v_dot2c_f32_bf16 v227, v51, v51\n"
        "v_lshrrev_b32 v248, 2, v252\n"
        "v_mul_u32_u24 v248, 896, v248\n"
        "v_and_b32 v128, 3, v252\n"
        "v_lshlrev_b32 v128, 7, v128\n"
        "v_add_u32 v248, v128, v248\n"
        "s_lshr_b32 s2, s3, 3\n"
        "s_mul_i32 s2, s2, 57344\n"
        "v_add_u32 v248, s2, v248\n"
        "s_mul_i32 s2, s70, 384\n"
        "v_add_u32 v248, s2, v248\n"
        "s_lshl_b32 s2, s68, 2\n"
        "s_add_u32 s2, s2, s71\n"
        "s_lshl_b32 s2, s2, 1\n"
        "s_add_u32 s2, s2, s65\n"
        "s_mul_i32 s2, s2, 3584\n"
        "v_and_b32 v128, 63, v252\n"
        "v_cmp_gt_u32 s[0:1], 28, v128\n"
        "v_lshlrev_b32 v128, 7, v128\n"
        "v_add_u32 v128, s2, v128\n"
        "v_cndmask_b32 v249, v130, v128, s[0:1]\n"
        "s_add_u32 m0, s73, 139264\n"
        "s_nop 0\n"
        "buffer_load_dwordx4 v12, s[40:43], s82 offen lds\n"
        "s_waitcnt lgkmcnt(6)\n"
        "v_dot2c_f32_bf16 v224, v52, v228\n"
        "v_dot2c_f32_bf16 v226, v52, v52\n"
        "v_dot2c_f32_bf16 v225, v53, v228\n"
        "v_dot2c_f32_bf16 v227, v53, v53\n"
        "v_dot2c_f32_bf16 v224, v54, v228\n"
        "v_dot2c_f32_bf16 v226, v54, v54\n"
        "v_dot2c_f32_bf16 v225, v55, v228\n"
        "v_dot2c_f32_bf16 v227, v55, v55\n"
        "s_waitcnt lgkmcnt(5)\n"
        "v_dot2c_f32_bf16 v224, v56, v228\n"
        "v_dot2c_f32_bf16 v226, v56, v56\n"
        "buffer_load_dwordx4 v13, s[40:43], s82 offen offset:1024 lds\n"
        "v_dot2c_f32_bf16 v225, v57, v228\n"
        "v_dot2c_f32_bf16 v227, v57, v57\n"
        "v_dot2c_f32_bf16 v224, v58, v228\n"
        "v_dot2c_f32_bf16 v226, v58, v58\n"
        "v_dot2c_f32_bf16 v225, v59, v228\n"
        "v_dot2c_f32_bf16 v227, v59, v59\n"
        "s_waitcnt lgkmcnt(4)\n"
        "v_dot2c_f32_bf16 v224, v60, v228\n"
        "v_dot2c_f32_bf16 v226, v60, v60\n"
        "v_dot2c_f32_bf16 v225, v61, v228\n"
        "v_dot2c_f32_bf16 v227, v61, v61\n"
        "v_dot2c_f32_bf16 v224, v62, v228\n"
        "s_add_u32 m0, s72, 147456\n"
        "s_nop 0\n"
        "buffer_load_dwordx4 v8, s[48:51], s89 offen lds\n"
        "v_dot2c_f32_bf16 v226, v62, v62\n"
        "v_dot2c_f32_bf16 v225, v63, v228\n"
        "v_dot2c_f32_bf16 v227, v63, v63\n"
        "s_waitcnt vmcnt(18)\n"
        "ds_read_b128 v[80:83], v2 offset:16384\n"
        "ds_read_b128 v[84:87], v3 offset:16384\n"
        "ds_read_b128 v[88:91], v4 offset:16384\n"
        "ds_read_b128 v[92:95], v5 offset:16384\n"
        "s_waitcnt lgkmcnt(7)\n"
        "v_dot2c_f32_bf16 v224, v64, v228\n"
        "v_dot2c_f32_bf16 v226, v64, v64\n"
        "v_dot2c_f32_bf16 v225, v65, v228\n"
        "buffer_load_dwordx4 v9, s[48:51], s89 offen offset:1024 lds\n"
        "v_dot2c_f32_bf16 v227, v65, v65\n"
        "v_dot2c_f32_bf16 v224, v66, v228\n"
        "v_dot2c_f32_bf16 v226, v66, v66\n"
        "v_dot2c_f32_bf16 v225, v67, v228\n"
        "v_dot2c_f32_bf16 v227, v67, v67\n"
        "s_waitcnt lgkmcnt(6)\n"
        "v_dot2c_f32_bf16 v224, v68, v228\n"
        "v_dot2c_f32_bf16 v226, v68, v68\n"
        "v_dot2c_f32_bf16 v225, v69, v228\n"
        "v_dot2c_f32_bf16 v227, v69, v69\n"
        "v_dot2c_f32_bf16 v224, v70, v228\n"
        "v_dot2c_f32_bf16 v226, v70, v70\n"
        "buffer_load_dwordx4 v10, s[48:51], s89 offen offset:2048 lds\n"
        "v_dot2c_f32_bf16 v225, v71, v228\n"
        "v_dot2c_f32_bf16 v227, v71, v71\n"
        "s_waitcnt lgkmcnt(5)\n"
        "v_dot2c_f32_bf16 v224, v72, v228\n"
        "v_dot2c_f32_bf16 v226, v72, v72\n"
        "v_dot2c_f32_bf16 v225, v73, v228\n"
        "v_dot2c_f32_bf16 v227, v73, v73\n"
        "v_dot2c_f32_bf16 v224, v74, v228\n"
        "v_dot2c_f32_bf16 v226, v74, v74\n"
        "v_dot2c_f32_bf16 v225, v75, v228\n"
        "v_dot2c_f32_bf16 v227, v75, v75\n"
        "s_waitcnt lgkmcnt(4)\n"
        "buffer_load_dwordx4 v11, s[48:51], s89 offen offset:3072 lds\n"
        "v_dot2c_f32_bf16 v224, v76, v228\n"
        "v_dot2c_f32_bf16 v226, v76, v76\n"
        "v_dot2c_f32_bf16 v225, v77, v228\n"
        "v_dot2c_f32_bf16 v227, v77, v77\n"
        "v_dot2c_f32_bf16 v224, v78, v228\n"
        "v_dot2c_f32_bf16 v226, v78, v78\n"
        "v_dot2c_f32_bf16 v225, v79, v228\n"
        "v_dot2c_f32_bf16 v227, v79, v79\n"
        "s_waitcnt vmcnt(17)\n"
        "ds_read_b128 v[96:99], v2 offset:20480\n"
        "ds_read_b128 v[100:103], v3 offset:20480\n"
        "ds_read_b128 v[104:107], v4 offset:20480\n"
        "ds_read_b128 v[108:111], v5 offset:20480\n"
        "s_waitcnt lgkmcnt(7)\n"
        "v_dot2c_f32_bf16 v224, v80, v228\n"
        "v_dot2c_f32_bf16 v226, v80, v80\n"
        "v_dot2c_f32_bf16 v225, v81, v228\n"
        "v_dot2c_f32_bf16 v227, v81, v81\n"
        "v_dot2c_f32_bf16 v224, v82, v228\n"
        "v_dot2c_f32_bf16 v226, v82, v82\n"
        "v_dot2c_f32_bf16 v225, v83, v228\n"
        "v_dot2c_f32_bf16 v227, v83, v83\n"
        "s_waitcnt lgkmcnt(6)\n"
        "v_dot2c_f32_bf16 v224, v84, v228\n"
        "v_dot2c_f32_bf16 v226, v84, v84\n"
        "v_dot2c_f32_bf16 v225, v85, v228\n"
        "v_dot2c_f32_bf16 v227, v85, v85\n"
        "v_dot2c_f32_bf16 v224, v86, v228\n"
        "v_dot2c_f32_bf16 v226, v86, v86\n"
        "v_dot2c_f32_bf16 v225, v87, v228\n"
        "v_dot2c_f32_bf16 v227, v87, v87\n"
        "s_waitcnt lgkmcnt(5)\n"
        "v_dot2c_f32_bf16 v224, v88, v228\n"
        "v_dot2c_f32_bf16 v226, v88, v88\n"
        "v_dot2c_f32_bf16 v225, v89, v228\n"
        "v_dot2c_f32_bf16 v227, v89, v89\n"
        "v_dot2c_f32_bf16 v224, v90, v228\n"
        "v_dot2c_f32_bf16 v226, v90, v90\n"
        "v_dot2c_f32_bf16 v225, v91, v228\n"
        "v_dot2c_f32_bf16 v227, v91, v91\n"
        "s_waitcnt lgkmcnt(4)\n"
        "v_dot2c_f32_bf16 v224, v92, v228\n"
        "v_dot2c_f32_bf16 v226, v92, v92\n"
        "v_dot2c_f32_bf16 v225, v93, v228\n"
        "v_dot2c_f32_bf16 v227, v93, v93\n"
        "v_dot2c_f32_bf16 v224, v94, v228\n"
        "v_dot2c_f32_bf16 v226, v94, v94\n"
        "v_dot2c_f32_bf16 v225, v95, v228\n"
        "v_dot2c_f32_bf16 v227, v95, v95\n"
        "s_waitcnt vmcnt(13)\n"
        "ds_read_b128 v[112:115], v2 offset:24576\n"
        "ds_read_b128 v[116:119], v3 offset:24576\n"
        "ds_read_b128 v[120:123], v4 offset:24576\n"
        "ds_read_b128 v[124:127], v5 offset:24576\n"
        "s_waitcnt lgkmcnt(7)\n"
        "v_dot2c_f32_bf16 v224, v96, v228\n"
        "v_dot2c_f32_bf16 v226, v96, v96\n"
        "v_dot2c_f32_bf16 v225, v97, v228\n"
        "v_dot2c_f32_bf16 v227, v97, v97\n"
        "v_dot2c_f32_bf16 v224, v98, v228\n"
        "v_dot2c_f32_bf16 v226, v98, v98\n"
        "v_dot2c_f32_bf16 v225, v99, v228\n"
        "v_dot2c_f32_bf16 v227, v99, v99\n"
        "s_waitcnt lgkmcnt(6)\n"
        "v_dot2c_f32_bf16 v224, v100, v228\n"
        "v_dot2c_f32_bf16 v226, v100, v100\n"
        "v_dot2c_f32_bf16 v225, v101, v228\n"
        "v_dot2c_f32_bf16 v227, v101, v101\n"
        "v_dot2c_f32_bf16 v224, v102, v228\n"
        "v_dot2c_f32_bf16 v226, v102, v102\n"
        "v_dot2c_f32_bf16 v225, v103, v228\n"
        "v_dot2c_f32_bf16 v227, v103, v103\n"
        "s_waitcnt lgkmcnt(5)\n"
        "v_dot2c_f32_bf16 v224, v104, v228\n"
        "v_dot2c_f32_bf16 v226, v104, v104\n"
        "v_dot2c_f32_bf16 v225, v105, v228\n"
        "v_dot2c_f32_bf16 v227, v105, v105\n"
        "v_dot2c_f32_bf16 v224, v106, v228\n"
        "v_dot2c_f32_bf16 v226, v106, v106\n"
        "v_dot2c_f32_bf16 v225, v107, v228\n"
        "v_dot2c_f32_bf16 v227, v107, v107\n"
        "s_waitcnt lgkmcnt(4)\n"
        "v_dot2c_f32_bf16 v224, v108, v228\n"
        "v_dot2c_f32_bf16 v226, v108, v108\n"
        "v_dot2c_f32_bf16 v225, v109, v228\n"
        "v_dot2c_f32_bf16 v227, v109, v109\n"
        "v_dot2c_f32_bf16 v224, v110, v228\n"
        "v_dot2c_f32_bf16 v226, v110, v110\n"
        "v_dot2c_f32_bf16 v225, v111, v228\n"
        "v_dot2c_f32_bf16 v227, v111, v111\n"
        "s_waitcnt lgkmcnt(3)\n"
        "v_dot2c_f32_bf16 v224, v112, v228\n"
        "v_dot2c_f32_bf16 v226, v112, v112\n"
        "v_dot2c_f32_bf16 v225, v113, v228\n"
        "v_dot2c_f32_bf16 v227, v113, v113\n"
        "v_dot2c_f32_bf16 v224, v114, v228\n"
        "v_dot2c_f32_bf16 v226, v114, v114\n"
        "v_dot2c_f32_bf16 v225, v115, v228\n"
        "v_dot2c_f32_bf16 v227, v115, v115\n"
        "s_waitcnt lgkmcnt(2)\n"
        "v_dot2c_f32_bf16 v224, v116, v228\n"
        "v_dot2c_f32_bf16 v226, v116, v116\n"
        "v_dot2c_f32_bf16 v225, v117, v228\n"
        "v_dot2c_f32_bf16 v227, v117, v117\n"
        "v_dot2c_f32_bf16 v224, v118, v228\n"
        "v_dot2c_f32_bf16 v226, v118, v118\n"
        "v_dot2c_f32_bf16 v225, v119, v228\n"
        "v_dot2c_f32_bf16 v227, v119, v119\n"
        "s_waitcnt lgkmcnt(1)\n"
        "v_dot2c_f32_bf16 v224, v120, v228\n"
        "v_dot2c_f32_bf16 v226, v120, v120\n"
        "v_dot2c_f32_bf16 v225, v121, v228\n"
        "v_dot2c_f32_bf16 v227, v121, v121\n"
        "v_dot2c_f32_bf16 v224, v122, v228\n"
        "v_dot2c_f32_bf16 v226, v122, v122\n"
        "v_dot2c_f32_bf16 v225, v123, v228\n"
        "v_dot2c_f32_bf16 v227, v123, v123\n"
        "s_waitcnt lgkmcnt(0)\n"
        "v_dot2c_f32_bf16 v224, v124, v228\n"
        "v_dot2c_f32_bf16 v226, v124, v124\n"
        "v_dot2c_f32_bf16 v225, v125, v228\n"
        "v_dot2c_f32_bf16 v227, v125, v125\n"
        "v_dot2c_f32_bf16 v224, v126, v228\n"
        "v_dot2c_f32_bf16 v226, v126, v126\n"
        "v_dot2c_f32_bf16 v225, v127, v228\n"
        "v_dot2c_f32_bf16 v227, v127, v127\n"
        "s_nop 2\n"
        "v_add_f32 v224, v224, v225\n"
        "v_add_f32 v226, v226, v227\n"
        "v_mov_b32 v225, v224\n"
        "v_mov_b32 v227, v226\n"
        "s_nop 0\n"
        "v_permlane32_swap_b32 v224, v225\n"
        "v_permlane32_swap_b32 v226, v227\n"
        "v_add_f32 v224, v224, v225\n"
        "v_add_f32 v226, v226, v227\n"
        "v_mov_b32 v229, 0x43e00000\n"
        "v_mul_f32 v225, 0x3b124925, v224\n"
        "v_fma_f32 v224, -v225, v229, v224\n"
        "v_mul_f32 v224, 0x3b124925, v224\n"
        "v_add_f32 v224, v225, v224\n"
        "v_mul_f32 v227, 0x3b124925, v226\n"
        "v_fma_f32 v226, -v227, v229, v226\n"
        "v_mul_f32 v226, 0x3b124925, v226\n"
        "v_add_f32 v226, v227, v226\n"
        "v_fma_f32 v226, -v224, v224, v226\n"
        "v_max_f32 v226, 0, v226\n"
        "v_cvt_pk_bf16_f32 v226, v226, v226\n"
        "v_lshlrev_b32 v226, 16, v226\n"
        "v_add_f32 v226, 0x3727c5ac, v226\n"
        "v_cvt_pk_bf16_f32 v226, v226, v226\n"
        "v_lshlrev_b32 v226, 16, v226\n"
        "v_sqrt_f32 v226, v226\n"
        "v_cvt_pk_bf16_f32 v224, v224, v224\n"
        "v_lshlrev_b32 v224, 16, v224\n"
        "v_cvt_pk_bf16_f32 v226, v226, v226\n"
        "v_lshlrev_b32 v226, 16, v226\n"
        "v_rcp_f32 v14, v226\n"
        "s_nop 0\n"
        "v_fma_f32 v229, -v226, v14, 1.0\n"
        "v_fma_f32 v14, v229, v14, v14\n"
        "v_mul_f32 v15, v224, v14\n"
        "v_xor_b32 v15, 0x80000000, v15\n"
        "s_mov_b32 s95, 0xffff0000\n"
        "v_lshlrev_b32 v224, 16, v16\n"
        "v_and_b32 v225, s95, v16\n"
        "v_fma_f32 v224, v224, v14, v15\n"
        "v_fma_f32 v225, v225, v14, v15\n"
        "v_cvt_pk_bf16_f32 v16, v224, v225\n"
        "v_lshlrev_b32 v226, 16, v17\n"
        "v_and_b32 v227, s95, v17\n"
        "v_fma_f32 v226, v226, v14, v15\n"
        "v_fma_f32 v227, v227, v14, v15\n"
        "v_cvt_pk_bf16_f32 v17, v226, v227\n"
        "v_lshlrev_b32 v224, 16, v18\n"
        "v_and_b32 v225, s95, v18\n"
        "v_fma_f32 v224, v224, v14, v15\n"
        "v_fma_f32 v225, v225, v14, v15\n"
        "v_cvt_pk_bf16_f32 v18, v224, v225\n"
        "v_lshlrev_b32 v226, 16, v19\n"
        "v_and_b32 v227, s95, v19\n"
        "v_fma_f32 v226, v226, v14, v15\n"
        "v_fma_f32 v227, v227, v14, v15\n"
        "v_cvt_pk_bf16_f32 v19, v226, v227\n"
        "s_mul_i32 s98, s64, 28672\n"
        "v_subrev_u32 v2, s98, v2\n"
        "v_subrev_u32 v3, s98, v3\n"
        "v_subrev_u32 v4, s98, v4\n"
        "v_subrev_u32 v5, s98, v5\n"
        "v_lshlrev_b32 v224, 16, v20\n"
        "v_and_b32 v225, s95, v20\n"
        "v_fma_f32 v224, v224, v14, v15\n"
        "v_fma_f32 v225, v225, v14, v15\n"
        "v_cvt_pk_bf16_f32 v20, v224, v225\n"
        "v_lshlrev_b32 v226, 16, v21\n"
        "v_and_b32 v227, s95, v21\n"
        "v_fma_f32 v226, v226, v14, v15\n"
        "v_fma_f32 v227, v227, v14, v15\n"
        "v_cvt_pk_bf16_f32 v21, v226, v227\n"
        "v_lshlrev_b32 v224, 16, v22\n"
        "v_and_b32 v225, s95, v22\n"
        "v_fma_f32 v224, v224, v14, v15\n"
        "v_fma_f32 v225, v225, v14, v15\n"
        "v_cvt_pk_bf16_f32 v22, v224, v225\n"
        "v_lshlrev_b32 v226, 16, v23\n"
        "v_and_b32 v227, s95, v23\n"
        "v_fma_f32 v226, v226, v14, v15\n"
        "v_fma_f32 v227, v227, v14, v15\n"
        "v_cvt_pk_bf16_f32 v23, v226, v227\n"
        "s_waitcnt vmcnt(7)\n"
        "s_barrier\n"
        "v_add_u32 v2, 65536, v2\n"
        "ds_read_b128 a[64:67], v2 offset:49152\n"
        "ds_read_b128 a[68:71], v2 offset:53248\n"
        "v_add_u32 v6, 65536, v6\n"
        "ds_read_b64_tr_b16 a[72:73], v6 offset:57344\n"
        "ds_read_b64_tr_b16 a[74:75], v6 offset:57856\n"
        "v_add_u32 v7, 65536, v7\n"
        "ds_read_b64_tr_b16 a[76:77], v7 offset:57344\n"
        "ds_read_b64_tr_b16 a[78:79], v7 offset:57856\n"
        "v_add_u32 v6, 65536, v6\n"
        "ds_read_b64_tr_b16 a[80:81], v6\n"
        "ds_read_b64_tr_b16 a[82:83], v6 offset:512\n"
        "v_add_u32 v7, 65536, v7\n"
        "ds_read_b64_tr_b16 a[84:85], v7\n"
        "ds_read_b64_tr_b16 a[86:87], v7 offset:512\n"
        "v_add_u32 v3, 65536, v3\n"
        "ds_read_b128 a[128:131], v3 offset:49152\n"
        "ds_read_b128 a[132:135], v3 offset:53248\n"
        "v_add_u32 v6, 4294901760, v6\n"
        "ds_read_b64_tr_b16 a[136:137], v6 offset:59392\n"
        "ds_read_b64_tr_b16 a[138:139], v6 offset:59904\n"
        "v_add_u32 v7, 4294901760, v7\n"
        "ds_read_b64_tr_b16 a[140:141], v7 offset:59392\n"
        "ds_read_b64_tr_b16 a[142:143], v7 offset:59904\n"
        "v_add_u32 v6, 65536, v6\n"
        "ds_read_b64_tr_b16 a[144:145], v6 offset:2048\n"
        "ds_read_b64_tr_b16 a[146:147], v6 offset:2560\n"
        "v_add_u32 v7, 65536, v7\n"
        "ds_read_b64_tr_b16 a[148:149], v7 offset:2048\n"
        "ds_read_b64_tr_b16 a[150:151], v7 offset:2560\n"
        "s_waitcnt lgkmcnt(10)\n"
        "v_mfma_f32_32x32x16_bf16 v[128:143], v[16:19], a[64:67], 0\n"
        "v_add_u32 v4, 65536, v4\n"
        "ds_read_b128 a[64:67], v4 offset:49152\n"
        "v_lshlrev_b32 v224, 16, v24\n"
        "v_and_b32 v225, s95, v24\n"
        "v_fma_f32 v224, v224, v14, v15\n"
        "s_add_u32 m0, s73, 0\n"
        "v_mfma_f32_32x32x16_bf16 v[144:159], v[16:19], a[68:71], 0\n"
        "ds_read_b128 a[68:71], v4 offset:53248\n"
        "buffer_load_dwordx4 v12, s[40:43], s83 offen lds\n"
        "v_fma_f32 v225, v225, v14, v15\n"
        "v_cvt_pk_bf16_f32 v24, v224, v225\n"
        "v_lshlrev_b32 v226, 16, v25\n"
        "v_and_b32 v227, s95, v25\n"
        "v_mfma_f32_32x32x16_bf16 a[0:15], a[72:75], v[16:19], 0\n"
        "v_add_u32 v6, 4294901760, v6\n"
        "ds_read_b64_tr_b16 a[72:73], v6 offset:61440\n"
        "ds_read_b64_tr_b16 a[74:75], v6 offset:61952\n"
        "v_fma_f32 v226, v226, v14, v15\n"
        "v_fma_f32 v227, v227, v14, v15\n"
        "v_cvt_pk_bf16_f32 v25, v226, v227\n"
        "v_mfma_f32_32x32x16_bf16 a[16:31], a[76:79], v[16:19], 0\n"
        "v_add_u32 v7, 4294901760, v7\n"
        "ds_read_b64_tr_b16 a[76:77], v7 offset:61440\n"
        "ds_read_b64_tr_b16 a[78:79], v7 offset:61952\n"
        "buffer_load_dwordx4 v13, s[40:43], s83 offen offset:1024 lds\n"
        "v_lshlrev_b32 v224, 16, v26\n"
        "v_and_b32 v225, s95, v26\n"
        "s_add_u32 m0, s72, 8192\n"
        "v_mfma_f32_32x32x16_bf16 a[32:47], a[80:83], v[16:19], 0\n"
        "v_add_u32 v6, 65536, v6\n"
        "ds_read_b64_tr_b16 a[80:81], v6 offset:4096\n"
        "ds_read_b64_tr_b16 a[82:83], v6 offset:4608\n"
        "v_fma_f32 v224, v224, v14, v15\n"
        "v_fma_f32 v225, v225, v14, v15\n"
        "v_cvt_pk_bf16_f32 v26, v224, v225\n"
        "v_mfma_f32_32x32x16_bf16 a[48:63], a[84:87], v[16:19], 0\n"
        "v_add_u32 v7, 65536, v7\n"
        "ds_read_b64_tr_b16 a[84:85], v7 offset:4096\n"
        "ds_read_b64_tr_b16 a[86:87], v7 offset:4608\n"
        "buffer_load_dwordx4 v8, s[48:51], s90 offen lds\n"
        "v_lshlrev_b32 v226, 16, v27\n"
        "v_and_b32 v227, s95, v27\n"
        "s_waitcnt lgkmcnt(10)\n"
        "v_mfma_f32_32x32x16_bf16 v[128:143], v[20:23], a[128:131], v[128:143]\n"
        "v_add_u32 v5, 65536, v5\n"
        "ds_read_b128 a[128:131], v5 offset:49152\n"
        "v_fma_f32 v226, v226, v14, v15\n"
        "v_fma_f32 v227, v227, v14, v15\n"
        "v_cvt_pk_bf16_f32 v27, v226, v227\n"
        "v_mfma_f32_32x32x16_bf16 v[144:159], v[20:23], a[132:135], v[144:159]\n"
        "ds_read_b128 a[132:135], v5 offset:53248\n"
        "buffer_load_dwordx4 v9, s[48:51], s90 offen offset:1024 lds\n"
        "v_lshlrev_b32 v224, 16, v28\n"
        "v_and_b32 v225, s95, v28\n"
        "v_fma_f32 v224, v224, v14, v15\n"
        "v_fma_f32 v225, v225, v14, v15\n"
        "v_mfma_f32_32x32x16_bf16 a[0:15], a[136:139], v[20:23], a[0:15]\n"
        "v_add_u32 v6, 4294901760, v6\n"
        "ds_read_b64_tr_b16 a[136:137], v6 offset:63488\n"
        "ds_read_b64_tr_b16 a[138:139], v6 offset:64000\n"
        "v_cvt_pk_bf16_f32 v28, v224, v225\n"
        "v_lshlrev_b32 v226, 16, v29\n"
        "v_and_b32 v227, s95, v29\n"
        "v_mfma_f32_32x32x16_bf16 a[16:31], a[140:143], v[20:23], a[16:31]\n"
        "v_add_u32 v7, 4294901760, v7\n"
        "ds_read_b64_tr_b16 a[140:141], v7 offset:63488\n"
        "ds_read_b64_tr_b16 a[142:143], v7 offset:64000\n"
        "buffer_load_dwordx4 v10, s[48:51], s90 offen offset:2048 lds\n"
        "v_fma_f32 v226, v226, v14, v15\n"
        "v_fma_f32 v227, v227, v14, v15\n"
        "v_mfma_f32_32x32x16_bf16 a[32:47], a[144:147], v[20:23], a[32:47]\n"
        "v_add_u32 v6, 65536, v6\n"
        "ds_read_b64_tr_b16 a[144:145], v6 offset:6144\n"
        "ds_read_b64_tr_b16 a[146:147], v6 offset:6656\n"
        "v_cvt_pk_bf16_f32 v29, v226, v227\n"
        "v_lshlrev_b32 v224, 16, v30\n"
        "v_and_b32 v225, s95, v30\n"
        "v_mfma_f32_32x32x16_bf16 a[48:63], a[148:151], v[20:23], a[48:63]\n"
        "v_add_u32 v7, 65536, v7\n"
        "ds_read_b64_tr_b16 a[148:149], v7 offset:6144\n"
        "ds_read_b64_tr_b16 a[150:151], v7 offset:6656\n"
        "buffer_load_dwordx4 v11, s[48:51], s90 offen offset:3072 lds\n"
        "v_fma_f32 v224, v224, v14, v15\n"
        "v_fma_f32 v225, v225, v14, v15\n"
        "s_add_u32 m0, s73, 24576\n"
        "s_waitcnt lgkmcnt(10)\n"
        "v_mfma_f32_32x32x16_bf16 v[128:143], v[24:27], a[64:67], v[128:143]\n"
        "v_cvt_pk_bf16_f32 v30, v224, v225\n"
        "v_lshlrev_b32 v226, 16, v31\n"
        "v_and_b32 v227, s95, v31\n"
        "v_fma_f32 v226, v226, v14, v15\n"
        "v_fma_f32 v227, v227, v14, v15\n"
        "v_mfma_f32_32x32x16_bf16 v[144:159], v[24:27], a[68:71], v[144:159]\n"
        "buffer_load_dwordx4 v12, s[40:43], s84 offen lds\n"
        "v_cvt_pk_bf16_f32 v31, v226, v227\n"
        "v_lshlrev_b32 v224, 16, v32\n"
        "v_and_b32 v225, s95, v32\n"
        "v_fma_f32 v224, v224, v14, v15\n"
        "v_fma_f32 v225, v225, v14, v15\n"
        "v_mfma_f32_32x32x16_bf16 a[0:15], a[72:75], v[24:27], a[0:15]\n"
        "s_waitcnt vmcnt(7) lgkmcnt(0)\n"
        "s_barrier\n"
        "v_add_u32 v2, 65536, v2\n"
        "ds_read_b128 a[64:67], v2 offset:8192\n"
        "ds_read_b128 a[68:71], v2 offset:12288\n"
        "ds_read_b64_tr_b16 a[72:73], v6 offset:16384\n"
        "ds_read_b64_tr_b16 a[74:75], v6 offset:16896\n"
        "buffer_load_dwordx4 v13, s[40:43], s84 offen offset:1024 lds\n"
        "s_add_u32 m0, s72, 32768\n"
        "v_mfma_f32_32x32x16_bf16 a[16:31], a[76:79], v[24:27], a[16:31]\n"
        "ds_read_b64_tr_b16 a[76:77], v7 offset:16384\n"
        "ds_read_b64_tr_b16 a[78:79], v7 offset:16896\n"
        "v_cvt_pk_bf16_f32 v32, v224, v225\n"
        "v_lshlrev_b32 v226, 16, v33\n"
        "v_and_b32 v227, s95, v33\n"
        "v_fma_f32 v226, v226, v14, v15\n"
        "v_mfma_f32_32x32x16_bf16 a[32:47], a[80:83], v[24:27], a[32:47]\n"
        "ds_read_b64_tr_b16 a[80:81], v6 offset:24576\n"
        "ds_read_b64_tr_b16 a[82:83], v6 offset:25088\n"
        "buffer_load_dwordx4 v8, s[48:51], s91 offen lds\n"
        "v_fma_f32 v227, v227, v14, v15\n"
        "v_cvt_pk_bf16_f32 v33, v226, v227\n"
        "v_lshlrev_b32 v224, 16, v34\n"
        "v_mfma_f32_32x32x16_bf16 a[48:63], a[84:87], v[24:27], a[48:63]\n"
        "ds_read_b64_tr_b16 a[84:85], v7 offset:24576\n"
        "ds_read_b64_tr_b16 a[86:87], v7 offset:25088\n"
        "v_and_b32 v225, s95, v34\n"
        "v_fma_f32 v224, v224, v14, v15\n"
        "v_fma_f32 v225, v225, v14, v15\n"
        "v_cvt_pk_bf16_f32 v34, v224, v225\n"
        "v_mfma_f32_32x32x16_bf16 v[128:143], v[28:31], a[128:131], v[128:143]\n"
        "v_add_u32 v3, 65536, v3\n"
        "ds_read_b128 a[128:131], v3 offset:8192\n"
        "buffer_load_dwordx4 v9, s[48:51], s91 offen offset:1024 lds\n"
        "v_lshlrev_b32 v226, 16, v35\n"
        "v_and_b32 v227, s95, v35\n"
        "v_fma_f32 v226, v226, v14, v15\n"
        "v_mfma_f32_32x32x16_bf16 v[144:159], v[28:31], a[132:135], v[144:159]\n"
        "ds_read_b128 a[132:135], v3 offset:12288\n"
        "v_fma_f32 v227, v227, v14, v15\n"
        "v_cvt_pk_bf16_f32 v35, v226, v227\n"
        "v_lshlrev_b32 v224, 16, v36\n"
        "v_and_b32 v225, s95, v36\n"
        "v_fma_f32 v224, v224, v14, v15\n"
        "v_mfma_f32_32x32x16_bf16 a[0:15], a[136:139], v[28:31], a[0:15]\n"
        "ds_read_b64_tr_b16 a[136:137], v6 offset:18432\n"
        "ds_read_b64_tr_b16 a[138:139], v6 offset:18944\n"
        "buffer_load_dwordx4 v10, s[48:51], s91 offen offset:2048 lds\n"
        "v_fma_f32 v225, v225, v14, v15\n"
        "v_cvt_pk_bf16_f32 v36, v224, v225\n"
        "v_lshlrev_b32 v226, 16, v37\n"
        "v_mfma_f32_32x32x16_bf16 a[16:31], a[140:143], v[28:31], a[16:31]\n"
        "ds_read_b64_tr_b16 a[140:141], v7 offset:18432\n"
        "ds_read_b64_tr_b16 a[142:143], v7 offset:18944\n"
        "v_and_b32 v227, s95, v37\n"
        "v_fma_f32 v226, v226, v14, v15\n"
        "v_fma_f32 v227, v227, v14, v15\n"
        "v_cvt_pk_bf16_f32 v37, v226, v227\n"
        "v_mfma_f32_32x32x16_bf16 a[32:47], a[144:147], v[28:31], a[32:47]\n"
        "ds_read_b64_tr_b16 a[144:145], v6 offset:26624\n"
        "ds_read_b64_tr_b16 a[146:147], v6 offset:27136\n"
        "buffer_load_dwordx4 v11, s[48:51], s91 offen offset:3072 lds\n"
        "v_lshlrev_b32 v224, 16, v38\n"
        "v_and_b32 v225, s95, v38\n"
        "v_fma_f32 v224, v224, v14, v15\n"
        "s_add_u32 m0, s73, 49152\n"
        "v_mfma_f32_32x32x16_bf16 a[48:63], a[148:151], v[28:31], a[48:63]\n"
        "ds_read_b64_tr_b16 a[148:149], v7 offset:26624\n"
        "ds_read_b64_tr_b16 a[150:151], v7 offset:27136\n"
        "v_fma_f32 v225, v225, v14, v15\n"
        "v_cvt_pk_bf16_f32 v38, v224, v225\n"
        "v_lshlrev_b32 v226, 16, v39\n"
        "v_and_b32 v227, s95, v39\n"
        "s_waitcnt lgkmcnt(10)\n"
        "v_mfma_f32_32x32x16_bf16 v[128:143], v[32:35], a[64:67], v[128:143]\n"
        "v_add_u32 v4, 65536, v4\n"
        "ds_read_b128 a[64:67], v4 offset:8192\n"
        "buffer_load_dwordx4 v12, s[40:43], s85 offen lds\n"
        "v_fma_f32 v226, v226, v14, v15\n"
        "v_fma_f32 v227, v227, v14, v15\n"
        "v_mfma_f32_32x32x16_bf16 v[144:159], v[32:35], a[68:71], v[144:159]\n"
        "ds_read_b128 a[68:71], v4 offset:12288\n"
        "v_cvt_pk_bf16_f32 v39, v226, v227\n"
        "v_lshlrev_b32 v224, 16, v40\n"
        "v_and_b32 v225, s95, v40\n"
        "v_fma_f32 v224, v224, v14, v15\n"
        "v_fma_f32 v225, v225, v14, v15\n"
        "v_mfma_f32_32x32x16_bf16 a[0:15], a[72:75], v[32:35], a[0:15]\n"
        "ds_read_b64_tr_b16 a[72:73], v6 offset:20480\n"
        "ds_read_b64_tr_b16 a[74:75], v6 offset:20992\n"
        "buffer_load_dwordx4 v13, s[40:43], s85 offen offset:1024 lds\n"
        "v_cvt_pk_bf16_f32 v40, v224, v225\n"
        "v_lshlrev_b32 v226, 16, v41\n"
        "v_and_b32 v227, s95, v41\n"
        "s_add_u32 m0, s72, 57344\n"
        "v_mfma_f32_32x32x16_bf16 a[16:31], a[76:79], v[32:35], a[16:31]\n"
        "ds_read_b64_tr_b16 a[76:77], v7 offset:20480\n"
        "ds_read_b64_tr_b16 a[78:79], v7 offset:20992\n"
        "v_fma_f32 v226, v226, v14, v15\n"
        "v_fma_f32 v227, v227, v14, v15\n"
        "v_cvt_pk_bf16_f32 v41, v226, v227\n"
        "v_lshlrev_b32 v224, 16, v42\n"
        "v_mfma_f32_32x32x16_bf16 a[32:47], a[80:83], v[32:35], a[32:47]\n"
        "ds_read_b64_tr_b16 a[80:81], v6 offset:28672\n"
        "ds_read_b64_tr_b16 a[82:83], v6 offset:29184\n"
        "buffer_load_dwordx4 v8, s[48:51], s92 offen lds\n"
        "v_and_b32 v225, s95, v42\n"
        "v_fma_f32 v224, v224, v14, v15\n"
        "v_fma_f32 v225, v225, v14, v15\n"
        "v_mfma_f32_32x32x16_bf16 a[48:63], a[84:87], v[32:35], a[48:63]\n"
        "ds_read_b64_tr_b16 a[84:85], v7 offset:28672\n"
        "ds_read_b64_tr_b16 a[86:87], v7 offset:29184\n"
        "buffer_load_dwordx4 v9, s[48:51], s92 offen offset:1024 lds\n"
        "v_cvt_pk_bf16_f32 v42, v224, v225\n"
        "v_lshlrev_b32 v226, 16, v43\n"
        "v_and_b32 v227, s95, v43\n"
        "s_waitcnt lgkmcnt(10)\n"
        "v_mfma_f32_32x32x16_bf16 v[128:143], v[36:39], a[128:131], v[128:143]\n"
        "v_add_u32 v5, 65536, v5\n"
        "ds_read_b128 a[128:131], v5 offset:8192\n"
        "v_fma_f32 v226, v226, v14, v15\n"
        "v_fma_f32 v227, v227, v14, v15\n"
        "v_cvt_pk_bf16_f32 v43, v226, v227\n"
        "v_mfma_f32_32x32x16_bf16 v[144:159], v[36:39], a[132:135], v[144:159]\n"
        "ds_read_b128 a[132:135], v5 offset:12288\n"
        "buffer_load_dwordx4 v10, s[48:51], s92 offen offset:2048 lds\n"
        "v_lshlrev_b32 v224, 16, v44\n"
        "v_and_b32 v225, s95, v44\n"
        "v_fma_f32 v224, v224, v14, v15\n"
        "v_fma_f32 v225, v225, v14, v15\n"
        "v_mfma_f32_32x32x16_bf16 a[0:15], a[136:139], v[36:39], a[0:15]\n"
        "ds_read_b64_tr_b16 a[136:137], v6 offset:22528\n"
        "ds_read_b64_tr_b16 a[138:139], v6 offset:23040\n"
        "v_cvt_pk_bf16_f32 v44, v224, v225\n"
        "v_lshlrev_b32 v226, 16, v45\n"
        "v_and_b32 v227, s95, v45\n"
        "v_fma_f32 v226, v226, v14, v15\n"
        "v_mfma_f32_32x32x16_bf16 a[16:31], a[140:143], v[36:39], a[16:31]\n"
        "ds_read_b64_tr_b16 a[140:141], v7 offset:22528\n"
        "ds_read_b64_tr_b16 a[142:143], v7 offset:23040\n"
        "buffer_load_dwordx4 v11, s[48:51], s92 offen offset:3072 lds\n"
        "v_fma_f32 v227, v227, v14, v15\n"
        "v_cvt_pk_bf16_f32 v45, v226, v227\n"
        "v_lshlrev_b32 v224, 16, v46\n"
        "s_add_u32 m0, s73, 73728\n"
        "v_mfma_f32_32x32x16_bf16 a[32:47], a[144:147], v[36:39], a[32:47]\n"
        "ds_read_b64_tr_b16 a[144:145], v6 offset:30720\n"
        "ds_read_b64_tr_b16 a[146:147], v6 offset:31232\n"
        "v_and_b32 v225, s95, v46\n"
        "v_fma_f32 v224, v224, v14, v15\n"
        "v_fma_f32 v225, v225, v14, v15\n"
        "v_cvt_pk_bf16_f32 v46, v224, v225\n"
        "v_mfma_f32_32x32x16_bf16 a[48:63], a[148:151], v[36:39], a[48:63]\n"
        "ds_read_b64_tr_b16 a[148:149], v7 offset:30720\n"
        "ds_read_b64_tr_b16 a[150:151], v7 offset:31232\n"
        "buffer_load_dwordx4 v12, s[40:43], s86 offen lds\n"
        "v_lshlrev_b32 v226, 16, v47\n"
        "v_and_b32 v227, s95, v47\n"
        "v_fma_f32 v226, v226, v14, v15\n"
        "s_waitcnt lgkmcnt(10)\n"
        "v_mfma_f32_32x32x16_bf16 v[128:143], v[40:43], a[64:67], v[128:143]\n"
        "v_fma_f32 v227, v227, v14, v15\n"
        "v_cvt_pk_bf16_f32 v47, v226, v227\n"
        "v_lshlrev_b32 v224, 16, v48\n"
        "v_and_b32 v225, s95, v48\n"
        "v_fma_f32 v224, v224, v14, v15\n"
        "v_mfma_f32_32x32x16_bf16 v[144:159], v[40:43], a[68:71], v[144:159]\n"
        "buffer_load_dwordx4 v13, s[40:43], s86 offen offset:1024 lds\n"
        "v_fma_f32 v225, v225, v14, v15\n"
        "v_cvt_pk_bf16_f32 v48, v224, v225\n"
        "v_lshlrev_b32 v226, 16, v49\n"
        "v_and_b32 v227, s95, v49\n"
        "v_fma_f32 v226, v226, v14, v15\n"
        "s_add_u32 m0, s72, 81920\n"
        "v_mfma_f32_32x32x16_bf16 a[0:15], a[72:75], v[40:43], a[0:15]\n"
        "s_waitcnt vmcnt(14) lgkmcnt(0)\n"
        "s_barrier\n"
        "v_add_u32 v2, 4294836224, v2\n"
        "ds_read_b128 a[64:67], v2\n"
        "ds_read_b128 a[68:71], v2 offset:4096\n"
        "v_add_u32 v6, 4294836224, v6\n"
        "ds_read_b64_tr_b16 a[72:73], v6 offset:8192\n"
        "ds_read_b64_tr_b16 a[74:75], v6 offset:8704\n"
        "v_mfma_f32_32x32x16_bf16 a[16:31], a[76:79], v[40:43], a[16:31]\n"
        "v_add_u32 v7, 4294836224, v7\n"
        "ds_read_b64_tr_b16 a[76:77], v7 offset:8192\n"
        "ds_read_b64_tr_b16 a[78:79], v7 offset:8704\n"
        "buffer_load_dwordx4 v8, s[48:51], s93 offen lds\n"
        "v_fma_f32 v227, v227, v14, v15\n"
        "v_cvt_pk_bf16_f32 v49, v226, v227\n"
        "v_mfma_f32_32x32x16_bf16 a[32:47], a[80:83], v[40:43], a[32:47]\n"
        "ds_read_b64_tr_b16 a[80:81], v6 offset:16384\n"
        "ds_read_b64_tr_b16 a[82:83], v6 offset:16896\n"
        "v_lshlrev_b32 v224, 16, v50\n"
        "v_and_b32 v225, s95, v50\n"
        "v_fma_f32 v224, v224, v14, v15\n"
        "v_fma_f32 v225, v225, v14, v15\n"
        "v_mfma_f32_32x32x16_bf16 a[48:63], a[84:87], v[40:43], a[48:63]\n"
        "ds_read_b64_tr_b16 a[84:85], v7 offset:16384\n"
        "ds_read_b64_tr_b16 a[86:87], v7 offset:16896\n"
        "buffer_load_dwordx4 v9, s[48:51], s93 offen offset:1024 lds\n"
        "v_cvt_pk_bf16_f32 v50, v224, v225\n"
        "v_lshlrev_b32 v226, 16, v51\n"
        "v_and_b32 v227, s95, v51\n"
        "v_mfma_f32_32x32x16_bf16 v[128:143], v[44:47], a[128:131], v[128:143]\n"
        "v_add_u32 v3, 4294836224, v3\n"
        "ds_read_b128 a[128:131], v3\n"
        "v_fma_f32 v226, v226, v14, v15\n"
        "v_fma_f32 v227, v227, v14, v15\n"
        "v_cvt_pk_bf16_f32 v51, v226, v227\n"
        "v_lshlrev_b32 v224, 16, v52\n"
        "v_mfma_f32_32x32x16_bf16 v[144:159], v[44:47], a[132:135], v[144:159]\n"
        "ds_read_b128 a[132:135], v3 offset:4096\n"
        "buffer_load_dwordx4 v10, s[48:51], s93 offen offset:2048 lds\n"
        "v_and_b32 v225, s95, v52\n"
        "v_fma_f32 v224, v224, v14, v15\n"
        "v_fma_f32 v225, v225, v14, v15\n"
        "v_cvt_pk_bf16_f32 v52, v224, v225\n"
        "v_mfma_f32_32x32x16_bf16 a[0:15], a[136:139], v[44:47], a[0:15]\n"
        "ds_read_b64_tr_b16 a[136:137], v6 offset:10240\n"
        "ds_read_b64_tr_b16 a[138:139], v6 offset:10752\n"
        "buffer_load_dwordx4 v11, s[48:51], s93 offen offset:3072 lds\n"
        "v_lshlrev_b32 v226, 16, v53\n"
        "v_and_b32 v227, s95, v53\n"
        "v_fma_f32 v226, v226, v14, v15\n"
        "s_add_u32 m0, s73, 98304\n"
        "v_mfma_f32_32x32x16_bf16 a[16:31], a[140:143], v[44:47], a[16:31]\n"
        "ds_read_b64_tr_b16 a[140:141], v7 offset:10240\n"
        "ds_read_b64_tr_b16 a[142:143], v7 offset:10752\n"
        "v_fma_f32 v227, v227, v14, v15\n"
        "v_cvt_pk_bf16_f32 v53, v226, v227\n"
        "v_lshlrev_b32 v224, 16, v54\n"
        "v_and_b32 v225, s95, v54\n"
        "v_mfma_f32_32x32x16_bf16 a[32:47], a[144:147], v[44:47], a[32:47]\n"
        "ds_read_b64_tr_b16 a[144:145], v6 offset:18432\n"
        "ds_read_b64_tr_b16 a[146:147], v6 offset:18944\n"
        "buffer_load_dwordx4 v12, s[40:43], s87 offen lds\n"
        "v_fma_f32 v224, v224, v14, v15\n"
        "v_fma_f32 v225, v225, v14, v15\n"
        "v_cvt_pk_bf16_f32 v54, v224, v225\n"
        "v_mfma_f32_32x32x16_bf16 a[48:63], a[148:151], v[44:47], a[48:63]\n"
        "ds_read_b64_tr_b16 a[148:149], v7 offset:18432\n"
        "ds_read_b64_tr_b16 a[150:151], v7 offset:18944\n"
        "v_lshlrev_b32 v226, 16, v55\n"
        "v_and_b32 v227, s95, v55\n"
        "v_fma_f32 v226, v226, v14, v15\n"
        "v_fma_f32 v227, v227, v14, v15\n"
        "s_waitcnt lgkmcnt(10)\n"
        "v_mfma_f32_32x32x16_bf16 v[128:143], v[48:51], a[64:67], v[128:143]\n"
        "v_add_u32 v4, 4294836224, v4\n"
        "ds_read_b128 a[64:67], v4\n"
        "buffer_load_dwordx4 v13, s[40:43], s87 offen offset:1024 lds\n"
        "v_cvt_pk_bf16_f32 v55, v226, v227\n"
        "v_lshlrev_b32 v224, 16, v56\n"
        "s_add_u32 m0, s72, 106496\n"
        "v_mfma_f32_32x32x16_bf16 v[144:159], v[48:51], a[68:71], v[144:159]\n"
        "ds_read_b128 a[68:71], v4 offset:4096\n"
        "v_and_b32 v225, s95, v56\n"
        "v_fma_f32 v224, v224, v14, v15\n"
        "v_fma_f32 v225, v225, v14, v15\n"
        "v_cvt_pk_bf16_f32 v56, v224, v225\n"
        "v_lshlrev_b32 v226, 16, v57\n"
        "v_mfma_f32_32x32x16_bf16 a[0:15], a[72:75], v[48:51], a[0:15]\n"
        "ds_read_b64_tr_b16 a[72:73], v6 offset:12288\n"
        "ds_read_b64_tr_b16 a[74:75], v6 offset:12800\n"
        "buffer_load_dwordx4 v8, s[48:51], s94 offen lds\n"
        "v_and_b32 v227, s95, v57\n"
        "v_fma_f32 v226, v226, v14, v15\n"
        "v_fma_f32 v227, v227, v14, v15\n"
        "v_mfma_f32_32x32x16_bf16 a[16:31], a[76:79], v[48:51], a[16:31]\n"
        "ds_read_b64_tr_b16 a[76:77], v7 offset:12288\n"
        "ds_read_b64_tr_b16 a[78:79], v7 offset:12800\n"
        "v_cvt_pk_bf16_f32 v57, v226, v227\n"
        "v_lshlrev_b32 v224, 16, v58\n"
        "v_and_b32 v225, s95, v58\n"
        "v_fma_f32 v224, v224, v14, v15\n"
        "v_mfma_f32_32x32x16_bf16 a[32:47], a[80:83], v[48:51], a[32:47]\n"
        "ds_read_b64_tr_b16 a[80:81], v6 offset:20480\n"
        "ds_read_b64_tr_b16 a[82:83], v6 offset:20992\n"
        "buffer_load_dwordx4 v9, s[48:51], s94 offen offset:1024 lds\n"
        "v_fma_f32 v225, v225, v14, v15\n"
        "v_cvt_pk_bf16_f32 v58, v224, v225\n"
        "v_lshlrev_b32 v226, 16, v59\n"
        "v_mfma_f32_32x32x16_bf16 a[48:63], a[84:87], v[48:51], a[48:63]\n"
        "ds_read_b64_tr_b16 a[84:85], v7 offset:20480\n"
        "ds_read_b64_tr_b16 a[86:87], v7 offset:20992\n"
        "v_and_b32 v227, s95, v59\n"
        "v_fma_f32 v226, v226, v14, v15\n"
        "v_fma_f32 v227, v227, v14, v15\n"
        "v_cvt_pk_bf16_f32 v59, v226, v227\n"
        "s_waitcnt lgkmcnt(10)\n"
        "v_mfma_f32_32x32x16_bf16 v[128:143], v[52:55], a[128:131], v[128:143]\n"
        "v_add_u32 v5, 4294836224, v5\n"
        "ds_read_b128 a[128:131], v5\n"
        "buffer_load_dwordx4 v10, s[48:51], s94 offen offset:2048 lds\n"
        "v_accvgpr_read_b32 v254, a113\n"
        "ds_write_b64 v254, v[14:15]\n"
        "v_mfma_f32_32x32x16_bf16 v[144:159], v[52:55], a[132:135], v[144:159]\n"
        "ds_read_b128 a[132:135], v5 offset:4096\n"
        "v_lshlrev_b32 v224, 16, v60\n"
        "v_and_b32 v225, s95, v60\n"
        "v_fma_f32 v224, v224, v14, v15\n"
        "v_fma_f32 v225, v225, v14, v15\n"
        "v_cvt_pk_bf16_f32 v60, v224, v225\n"
        "v_mfma_f32_32x32x16_bf16 a[0:15], a[136:139], v[52:55], a[0:15]\n"
        "ds_read_b64_tr_b16 a[136:137], v6 offset:14336\n"
        "ds_read_b64_tr_b16 a[138:139], v6 offset:14848\n"
        "buffer_load_dwordx4 v11, s[48:51], s94 offen offset:3072 lds\n"
        "v_lshlrev_b32 v226, 16, v61\n"
        "v_and_b32 v227, s95, v61\n"
        "v_fma_f32 v226, v226, v14, v15\n"
        "v_mfma_f32_32x32x16_bf16 a[16:31], a[140:143], v[52:55], a[16:31]\n"
        "ds_read_b64_tr_b16 a[140:141], v7 offset:14336\n"
        "ds_read_b64_tr_b16 a[142:143], v7 offset:14848\n"
        "v_fma_f32 v227, v227, v14, v15\n"
        "v_cvt_pk_bf16_f32 v61, v226, v227\n"
        "v_lshlrev_b32 v224, 16, v62\n"
        "v_and_b32 v225, s95, v62\n"
        "v_mfma_f32_32x32x16_bf16 a[32:47], a[144:147], v[52:55], a[32:47]\n"
        "ds_read_b64_tr_b16 a[144:145], v6 offset:22528\n"
        "ds_read_b64_tr_b16 a[146:147], v6 offset:23040\n"
        "v_add_u32 v8, 128, v8\n"
        "v_add_u32 v9, 128, v9\n"
        "v_add_u32 v10, 128, v10\n"
        "v_add_u32 v11, 128, v11\n"
        "s_add_u32 m0, s72, 131072\n"
        "s_nop 0\n"
        "buffer_load_dwordx4 v8, s[48:51], s88 offen lds\n"
        "v_mfma_f32_32x32x16_bf16 a[48:63], a[148:151], v[52:55], a[48:63]\n"
        "ds_read_b64_tr_b16 a[148:149], v7 offset:22528\n"
        "ds_read_b64_tr_b16 a[150:151], v7 offset:23040\n"
        "buffer_load_dwordx4 v9, s[48:51], s88 offen offset:1024 lds\n"
        "v_fma_f32 v224, v224, v14, v15\n"
        "v_fma_f32 v225, v225, v14, v15\n"
        "v_cvt_pk_bf16_f32 v62, v224, v225\n"
        "s_waitcnt lgkmcnt(11)\n"
        "v_mfma_f32_32x32x16_bf16 v[128:143], v[56:59], a[64:67], v[128:143]\n"
        "v_lshlrev_b32 v226, 16, v63\n"
        "v_and_b32 v227, s95, v63\n"
        "v_fma_f32 v226, v226, v14, v15\n"
        "v_fma_f32 v227, v227, v14, v15\n"
        "v_cvt_pk_bf16_f32 v63, v226, v227\n"
        "v_mfma_f32_32x32x16_bf16 v[144:159], v[56:59], a[68:71], v[144:159]\n"
        "buffer_load_dwordx4 v10, s[48:51], s88 offen offset:2048 lds\n"
        "v_lshlrev_b32 v224, 16, v64\n"
        "v_and_b32 v225, s95, v64\n"
        "v_fma_f32 v224, v224, v14, v15\n"
        "v_fma_f32 v225, v225, v14, v15\n"
        "v_cvt_pk_bf16_f32 v64, v224, v225\n"
        "v_mfma_f32_32x32x16_bf16 a[0:15], a[72:75], v[56:59], a[0:15]\n"
        "s_waitcnt vmcnt(21) lgkmcnt(0)\n"
        "s_barrier\n"
        "ds_read_b128 a[64:67], v2 offset:24576\n"
        "ds_read_b128 a[68:71], v2 offset:28672\n"
        "ds_read_b64_tr_b16 a[72:73], v6 offset:32768\n"
        "ds_read_b64_tr_b16 a[74:75], v6 offset:33280\n"
        "v_mfma_f32_32x32x16_bf16 a[16:31], a[76:79], v[56:59], a[16:31]\n"
        "ds_read_b64_tr_b16 a[76:77], v7 offset:32768\n"
        "ds_read_b64_tr_b16 a[78:79], v7 offset:33280\n"
        "buffer_load_dwordx4 v11, s[48:51], s88 offen offset:3072 lds\n"
        "v_lshlrev_b32 v226, 16, v65\n"
        "v_and_b32 v227, s95, v65\n"
        "v_fma_f32 v226, v226, v14, v15\n"
        "s_add_u32 m0, s72, 0\n"
        "v_mfma_f32_32x32x16_bf16 a[32:47], a[80:83], v[56:59], a[32:47]\n"
        "ds_read_b64_tr_b16 a[80:81], v6 offset:40960\n"
        "ds_read_b64_tr_b16 a[82:83], v6 offset:41472\n"
        "v_fma_f32 v227, v227, v14, v15\n"
        "v_cvt_pk_bf16_f32 v65, v226, v227\n"
        "v_lshlrev_b32 v224, 16, v66\n"
        "v_and_b32 v225, s95, v66\n"
        "v_mfma_f32_32x32x16_bf16 a[48:63], a[84:87], v[56:59], a[48:63]\n"
        "ds_read_b64_tr_b16 a[84:85], v7 offset:40960\n"
        "ds_read_b64_tr_b16 a[86:87], v7 offset:41472\n"
        "buffer_load_dwordx4 v8, s[48:51], s89 offen lds\n"
        "v_fma_f32 v224, v224, v14, v15\n"
        "v_fma_f32 v225, v225, v14, v15\n"
        "v_cvt_pk_bf16_f32 v66, v224, v225\n"
        "v_mfma_f32_32x32x16_bf16 v[128:143], v[60:63], a[128:131], v[128:143]\n"
        "ds_read_b128 a[128:131], v3 offset:24576\n"
        "v_lshlrev_b32 v226, 16, v67\n"
        "v_and_b32 v227, s95, v67\n"
        "v_fma_f32 v226, v226, v14, v15\n"
        "v_fma_f32 v227, v227, v14, v15\n"
        "v_cvt_pk_bf16_f32 v67, v226, v227\n"
        "v_mfma_f32_32x32x16_bf16 v[144:159], v[60:63], a[132:135], v[144:159]\n"
        "ds_read_b128 a[132:135], v3 offset:28672\n"
        "buffer_load_dwordx4 v9, s[48:51], s89 offen offset:1024 lds\n"
        "v_lshlrev_b32 v224, 16, v68\n"
        "v_and_b32 v225, s95, v68\n"
        "v_fma_f32 v224, v224, v14, v15\n"
        "v_fma_f32 v225, v225, v14, v15\n"
        "v_mfma_f32_32x32x16_bf16 a[0:15], a[136:139], v[60:63], a[0:15]\n"
        "ds_read_b64_tr_b16 a[136:137], v6 offset:34816\n"
        "ds_read_b64_tr_b16 a[138:139], v6 offset:35328\n"
        "v_cvt_pk_bf16_f32 v68, v224, v225\n"
        "v_lshlrev_b32 v226, 16, v69\n"
        "v_and_b32 v227, s95, v69\n"
        "v_fma_f32 v226, v226, v14, v15\n"
        "v_mfma_f32_32x32x16_bf16 a[16:31], a[140:143], v[60:63], a[16:31]\n"
        "ds_read_b64_tr_b16 a[140:141], v7 offset:34816\n"
        "ds_read_b64_tr_b16 a[142:143], v7 offset:35328\n"
        "buffer_load_dwordx4 v10, s[48:51], s89 offen offset:2048 lds\n"
        "v_fma_f32 v227, v227, v14, v15\n"
        "v_cvt_pk_bf16_f32 v69, v226, v227\n"
        "v_lshlrev_b32 v224, 16, v70\n"
        "v_mfma_f32_32x32x16_bf16 a[32:47], a[144:147], v[60:63], a[32:47]\n"
        "ds_read_b64_tr_b16 a[144:145], v6 offset:43008\n"
        "ds_read_b64_tr_b16 a[146:147], v6 offset:43520\n"
        "v_and_b32 v225, s95, v70\n"
        "v_fma_f32 v224, v224, v14, v15\n"
        "v_fma_f32 v225, v225, v14, v15\n"
        "v_cvt_pk_bf16_f32 v70, v224, v225\n"
        "v_mfma_f32_32x32x16_bf16 a[48:63], a[148:151], v[60:63], a[48:63]\n"
        "ds_read_b64_tr_b16 a[148:149], v7 offset:43008\n"
        "ds_read_b64_tr_b16 a[150:151], v7 offset:43520\n"
        "buffer_load_dwordx4 v11, s[48:51], s89 offen offset:3072 lds\n"
        "v_lshlrev_b32 v226, 16, v71\n"
        "v_and_b32 v227, s95, v71\n"
        "v_fma_f32 v226, v226, v14, v15\n"
        "s_waitcnt lgkmcnt(10)\n"
        "v_mfma_f32_32x32x16_bf16 v[128:143], v[64:67], a[64:67], v[128:143]\n"
        "ds_read_b128 a[64:67], v4 offset:24576\n"
        "v_fma_f32 v227, v227, v14, v15\n"
        "v_cvt_pk_bf16_f32 v71, v226, v227\n"
        "v_lshlrev_b32 v224, 16, v72\n"
        "v_and_b32 v225, s95, v72\n"
        "v_mfma_f32_32x32x16_bf16 v[144:159], v[64:67], a[68:71], v[144:159]\n"
        "ds_read_b128 a[68:71], v4 offset:28672\n"
        "v_fma_f32 v224, v224, v14, v15\n"
        "v_fma_f32 v225, v225, v14, v15\n"
        "v_cvt_pk_bf16_f32 v72, v224, v225\n"
        "v_lshlrev_b32 v226, 16, v73\n"
        "v_and_b32 v227, s95, v73\n"
        "v_mfma_f32_32x32x16_bf16 a[0:15], a[72:75], v[64:67], a[0:15]\n"
        "ds_read_b64_tr_b16 a[72:73], v6 offset:36864\n"
        "ds_read_b64_tr_b16 a[74:75], v6 offset:37376\n"
        "v_fma_f32 v226, v226, v14, v15\n"
        "v_fma_f32 v227, v227, v14, v15\n"
        "v_cvt_pk_bf16_f32 v73, v226, v227\n"
        "v_lshlrev_b32 v224, 16, v74\n"
        "v_mfma_f32_32x32x16_bf16 a[16:31], a[76:79], v[64:67], a[16:31]\n"
        "ds_read_b64_tr_b16 a[76:77], v7 offset:36864\n"
        "ds_read_b64_tr_b16 a[78:79], v7 offset:37376\n"
        "v_and_b32 v225, s95, v74\n"
        "v_fma_f32 v224, v224, v14, v15\n"
        "v_fma_f32 v225, v225, v14, v15\n"
        "v_cvt_pk_bf16_f32 v74, v224, v225\n"
        "v_mfma_f32_32x32x16_bf16 a[32:47], a[80:83], v[64:67], a[32:47]\n"
        "ds_read_b64_tr_b16 a[80:81], v6 offset:45056\n"
        "ds_read_b64_tr_b16 a[82:83], v6 offset:45568\n"
        "v_lshlrev_b32 v226, 16, v75\n"
        "v_and_b32 v227, s95, v75\n"
        "v_fma_f32 v226, v226, v14, v15\n"
        "v_fma_f32 v227, v227, v14, v15\n"
        "v_mfma_f32_32x32x16_bf16 a[48:63], a[84:87], v[64:67], a[48:63]\n"
        "ds_read_b64_tr_b16 a[84:85], v7 offset:45056\n"
        "ds_read_b64_tr_b16 a[86:87], v7 offset:45568\n"
        "v_cvt_pk_bf16_f32 v75, v226, v227\n"
        "v_lshlrev_b32 v224, 16, v76\n"
        "v_and_b32 v225, s95, v76\n"
        "v_fma_f32 v224, v224, v14, v15\n"
        "s_waitcnt lgkmcnt(10)\n"
        "v_mfma_f32_32x32x16_bf16 v[128:143], v[68:71], a[128:131], v[128:143]\n"
        "ds_read_b128 a[128:131], v5 offset:24576\n"
        "v_fma_f32 v225, v225, v14, v15\n"
        "v_cvt_pk_bf16_f32 v76, v224, v225\n"
        "v_lshlrev_b32 v226, 16, v77\n"
        "v_and_b32 v227, s95, v77\n"
        "v_mfma_f32_32x32x16_bf16 v[144:159], v[68:71], a[132:135], v[144:159]\n"
        "ds_read_b128 a[132:135], v5 offset:28672\n"
        "v_fma_f32 v226, v226, v14, v15\n"
        "v_fma_f32 v227, v227, v14, v15\n"
        "v_cvt_pk_bf16_f32 v77, v226, v227\n"
        "v_lshlrev_b32 v224, 16, v78\n"
        "v_and_b32 v225, s95, v78\n"
        "v_mfma_f32_32x32x16_bf16 a[0:15], a[136:139], v[68:71], a[0:15]\n"
        "ds_read_b64_tr_b16 a[136:137], v6 offset:38912\n"
        "ds_read_b64_tr_b16 a[138:139], v6 offset:39424\n"
        "v_fma_f32 v224, v224, v14, v15\n"
        "v_fma_f32 v225, v225, v14, v15\n"
        "v_cvt_pk_bf16_f32 v78, v224, v225\n"
        "v_lshlrev_b32 v226, 16, v79\n"
        "v_mfma_f32_32x32x16_bf16 a[16:31], a[140:143], v[68:71], a[16:31]\n"
        "ds_read_b64_tr_b16 a[140:141], v7 offset:38912\n"
        "ds_read_b64_tr_b16 a[142:143], v7 offset:39424\n"
        "v_and_b32 v227, s95, v79\n"
        "v_fma_f32 v226, v226, v14, v15\n"
        "v_fma_f32 v227, v227, v14, v15\n"
        "v_cvt_pk_bf16_f32 v79, v226, v227\n"
        "v_mfma_f32_32x32x16_bf16 a[32:47], a[144:147], v[68:71], a[32:47]\n"
        "ds_read_b64_tr_b16 a[144:145], v6 offset:47104\n"
        "ds_read_b64_tr_b16 a[146:147], v6 offset:47616\n"
        "v_lshlrev_b32 v224, 16, v80\n"
        "v_and_b32 v225, s95, v80\n"
        "v_fma_f32 v224, v224, v14, v15\n"
        "v_fma_f32 v225, v225, v14, v15\n"
        "v_mfma_f32_32x32x16_bf16 a[48:63], a[148:151], v[68:71], a[48:63]\n"
        "ds_read_b64_tr_b16 a[148:149], v7 offset:47104\n"
        "ds_read_b64_tr_b16 a[150:151], v7 offset:47616\n"
        "v_cvt_pk_bf16_f32 v80, v224, v225\n"
        "v_lshlrev_b32 v226, 16, v81\n"
        "v_and_b32 v227, s95, v81\n"
        "v_fma_f32 v226, v226, v14, v15\n"
        "s_waitcnt lgkmcnt(10)\n"
        "v_mfma_f32_32x32x16_bf16 v[128:143], v[72:75], a[64:67], v[128:143]\n"
        "v_fma_f32 v227, v227, v14, v15\n"
        "v_cvt_pk_bf16_f32 v81, v226, v227\n"
        "v_lshlrev_b32 v224, 16, v82\n"
        "v_and_b32 v225, s95, v82\n"
        "v_fma_f32 v224, v224, v14, v15\n"
        "v_mfma_f32_32x32x16_bf16 v[144:159], v[72:75], a[68:71], v[144:159]\n"
        "v_fma_f32 v225, v225, v14, v15\n"
        "v_cvt_pk_bf16_f32 v82, v224, v225\n"
        "v_lshlrev_b32 v226, 16, v83\n"
        "v_and_b32 v227, s95, v83\n"
        "v_fma_f32 v226, v226, v14, v15\n"
        "v_fma_f32 v227, v227, v14, v15\n"
        "v_mfma_f32_32x32x16_bf16 a[0:15], a[72:75], v[72:75], a[0:15]\n"
        "s_waitcnt vmcnt(20) lgkmcnt(0)\n"
        "s_barrier\n"
        "ds_read_b128 a[64:67], v2 offset:49152\n"
        "ds_read_b128 a[68:71], v2 offset:53248\n"
        "ds_read_b64_tr_b16 a[72:73], v6 offset:57344\n"
        "ds_read_b64_tr_b16 a[74:75], v6 offset:57856\n"
        "s_add_u32 m0, s72, 16384\n"
        "s_nop 0\n"
        "buffer_load_dwordx4 v8, s[48:51], s90 offen lds\n"
        "v_mfma_f32_32x32x16_bf16 a[16:31], a[76:79], v[72:75], a[16:31]\n"
        "ds_read_b64_tr_b16 a[76:77], v7 offset:57344\n"
        "ds_read_b64_tr_b16 a[78:79], v7 offset:57856\n"
        "buffer_load_dwordx4 v9, s[48:51], s90 offen offset:1024 lds\n"
        "v_cvt_pk_bf16_f32 v83, v226, v227\n"
        "v_lshlrev_b32 v224, 16, v84\n"
        "v_and_b32 v225, s95, v84\n"
        "v_mfma_f32_32x32x16_bf16 a[32:47], a[80:83], v[72:75], a[32:47]\n"
        "v_add_u32 v6, 65536, v6\n"
        "ds_read_b64_tr_b16 a[80:81], v6\n"
        "ds_read_b64_tr_b16 a[82:83], v6 offset:512\n"
        "v_fma_f32 v224, v224, v14, v15\n"
        "v_fma_f32 v225, v225, v14, v15\n"
        "v_cvt_pk_bf16_f32 v84, v224, v225\n"
        "v_mfma_f32_32x32x16_bf16 a[48:63], a[84:87], v[72:75], a[48:63]\n"
        "v_add_u32 v7, 65536, v7\n"
        "ds_read_b64_tr_b16 a[84:85], v7\n"
        "ds_read_b64_tr_b16 a[86:87], v7 offset:512\n"
        "buffer_load_dwordx4 v10, s[48:51], s90 offen offset:2048 lds\n"
        "v_lshlrev_b32 v226, 16, v85\n"
        "v_and_b32 v227, s95, v85\n"
        "v_mfma_f32_32x32x16_bf16 v[128:143], v[76:79], a[128:131], v[128:143]\n"
        "ds_read_b128 a[128:131], v3 offset:49152\n"
        "v_fma_f32 v226, v226, v14, v15\n"
        "v_fma_f32 v227, v227, v14, v15\n"
        "v_cvt_pk_bf16_f32 v85, v226, v227\n"
        "v_lshlrev_b32 v224, 16, v86\n"
        "v_and_b32 v225, s95, v86\n"
        "v_mfma_f32_32x32x16_bf16 v[144:159], v[76:79], a[132:135], v[144:159]\n"
        "ds_read_b128 a[132:135], v3 offset:53248\n"
        "buffer_load_dwordx4 v11, s[48:51], s90 offen offset:3072 lds\n"
        "v_fma_f32 v224, v224, v14, v15\n"
        "v_fma_f32 v225, v225, v14, v15\n"
        "v_cvt_pk_bf16_f32 v86, v224, v225\n"
        "v_lshlrev_b32 v226, 16, v87\n"
        "s_add_u32 m0, s72, 32768\n"
        "v_mfma_f32_32x32x16_bf16 a[0:15], a[136:139], v[76:79], a[0:15]\n"
        "v_add_u32 v6, 4294901760, v6\n"
        "ds_read_b64_tr_b16 a[136:137], v6 offset:59392\n"
        "ds_read_b64_tr_b16 a[138:139], v6 offset:59904\n"
        "v_and_b32 v227, s95, v87\n"
        "v_fma_f32 v226, v226, v14, v15\n"
        "v_fma_f32 v227, v227, v14, v15\n"
        "v_mfma_f32_32x32x16_bf16 a[16:31], a[140:143], v[76:79], a[16:31]\n"
        "v_add_u32 v7, 4294901760, v7\n"
        "ds_read_b64_tr_b16 a[140:141], v7 offset:59392\n"
        "ds_read_b64_tr_b16 a[142:143], v7 offset:59904\n"
        "buffer_load_dwordx4 v8, s[48:51], s91 offen lds\n"
        "v_cvt_pk_bf16_f32 v87, v226, v227\n"
        "v_lshlrev_b32 v224, 16, v88\n"
        "v_mfma_f32_32x32x16_bf16 a[32:47], a[144:147], v[76:79], a[32:47]\n"
        "v_add_u32 v6, 65536, v6\n"
        "ds_read_b64_tr_b16 a[144:145], v6 offset:2048\n"
        "ds_read_b64_tr_b16 a[146:147], v6 offset:2560\n"
        "v_and_b32 v225, s95, v88\n"
        "v_fma_f32 v224, v224, v14, v15\n"
        "v_fma_f32 v225, v225, v14, v15\n"
        "v_mfma_f32_32x32x16_bf16 a[48:63], a[148:151], v[76:79], a[48:63]\n"
        "v_add_u32 v7, 65536, v7\n"
        "ds_read_b64_tr_b16 a[148:149], v7 offset:2048\n"
        "ds_read_b64_tr_b16 a[150:151], v7 offset:2560\n"
        "buffer_load_dwordx4 v9, s[48:51], s91 offen offset:1024 lds\n"
        "v_cvt_pk_bf16_f32 v88, v224, v225\n"
        "v_lshlrev_b32 v226, 16, v89\n"
        "s_waitcnt lgkmcnt(10)\n"
        "v_mfma_f32_32x32x16_bf16 v[128:143], v[80:83], a[64:67], v[128:143]\n"
        "ds_read_b128 a[64:67], v4 offset:49152\n"
        "v_and_b32 v227, s95, v89\n"
        "v_fma_f32 v226, v226, v14, v15\n"
        "v_fma_f32 v227, v227, v14, v15\n"
        "v_cvt_pk_bf16_f32 v89, v226, v227\n"
        "v_mfma_f32_32x32x16_bf16 v[144:159], v[80:83], a[68:71], v[144:159]\n"
        "ds_read_b128 a[68:71], v4 offset:53248\n"
        "buffer_load_dwordx4 v10, s[48:51], s91 offen offset:2048 lds\n"
        "v_lshlrev_b32 v224, 16, v90\n"
        "v_and_b32 v225, s95, v90\n"
        "v_fma_f32 v224, v224, v14, v15\n"
        "v_fma_f32 v225, v225, v14, v15\n"
        "v_mfma_f32_32x32x16_bf16 a[0:15], a[72:75], v[80:83], a[0:15]\n"
        "v_add_u32 v6, 4294901760, v6\n"
        "ds_read_b64_tr_b16 a[72:73], v6 offset:61440\n"
        "ds_read_b64_tr_b16 a[74:75], v6 offset:61952\n"
        "v_cvt_pk_bf16_f32 v90, v224, v225\n"
        "v_lshlrev_b32 v226, 16, v91\n"
        "v_and_b32 v227, s95, v91\n"
        "v_mfma_f32_32x32x16_bf16 a[16:31], a[76:79], v[80:83], a[16:31]\n"
        "v_add_u32 v7, 4294901760, v7\n"
        "ds_read_b64_tr_b16 a[76:77], v7 offset:61440\n"
        "ds_read_b64_tr_b16 a[78:79], v7 offset:61952\n"
        "buffer_load_dwordx4 v11, s[48:51], s91 offen offset:3072 lds\n"
        "v_fma_f32 v226, v226, v14, v15\n"
        "v_fma_f32 v227, v227, v14, v15\n"
        "v_bfe_u32 v250, v252, 4, 2\n"
        "v_xor_b32 v250, 0, v250\n"
        "v_lshl_add_u32 v250, v250, 28, v248\n"
        "v_mfma_f32_32x32x16_bf16 a[32:47], a[80:83], v[80:83], a[32:47]\n"
        "v_add_u32 v6, 65536, v6\n"
        "ds_read_b64_tr_b16 a[80:81], v6 offset:4096\n"
        "ds_read_b64_tr_b16 a[82:83], v6 offset:4608\n"
        "v_cvt_pk_bf16_f32 v91, v226, v227\n"
        "v_lshlrev_b32 v224, 16, v92\n"
        "v_and_b32 v225, s95, v92\n"
        "v_mfma_f32_32x32x16_bf16 a[48:63], a[84:87], v[80:83], a[48:63]\n"
        "v_add_u32 v7, 65536, v7\n"
        "ds_read_b64_tr_b16 a[84:85], v7 offset:4096\n"
        "ds_read_b64_tr_b16 a[86:87], v7 offset:4608\n"
        "buffer_load_dword v251, v250, s[52:55], 0 offen\n"
        "v_fma_f32 v224, v224, v14, v15\n"
        "v_fma_f32 v225, v225, v14, v15\n"
        "v_bfe_u32 v250, v252, 4, 2\n"
        "v_xor_b32 v250, 1, v250\n"
        "v_lshl_add_u32 v250, v250, 28, v248\n"
        "s_waitcnt lgkmcnt(10)\n"
        "v_mfma_f32_32x32x16_bf16 v[128:143], v[84:87], a[128:131], v[128:143]\n"
        "ds_read_b128 a[128:131], v5 offset:49152\n"
        "buffer_load_dword v251, v250, s[52:55], 0 offen\n"
        "v_cvt_pk_bf16_f32 v92, v224, v225\n"
        "v_lshlrev_b32 v226, 16, v93\n"
        "v_and_b32 v227, s95, v93\n"
        "v_mfma_f32_32x32x16_bf16 v[144:159], v[84:87], a[132:135], v[144:159]\n"
        "ds_read_b128 a[132:135], v5 offset:53248\n"
        "v_fma_f32 v226, v226, v14, v15\n"
        "v_fma_f32 v227, v227, v14, v15\n"
        "v_cvt_pk_bf16_f32 v93, v226, v227\n"
        "v_lshlrev_b32 v224, 16, v94\n"
        "v_and_b32 v225, s95, v94\n"
        "v_mfma_f32_32x32x16_bf16 a[0:15], a[136:139], v[84:87], a[0:15]\n"
        "v_add_u32 v6, 4294901760, v6\n"
        "ds_read_b64_tr_b16 a[136:137], v6 offset:63488\n"
        "ds_read_b64_tr_b16 a[138:139], v6 offset:64000\n"
        "v_fma_f32 v224, v224, v14, v15\n"
        "v_fma_f32 v225, v225, v14, v15\n"
        "v_cvt_pk_bf16_f32 v94, v224, v225\n"
        "v_mfma_f32_32x32x16_bf16 a[16:31], a[140:143], v[84:87], a[16:31]\n"
        "v_add_u32 v7, 4294901760, v7\n"
        "ds_read_b64_tr_b16 a[140:141], v7 offset:63488\n"
        "ds_read_b64_tr_b16 a[142:143], v7 offset:64000\n"
        "v_lshlrev_b32 v226, 16, v95\n"
        "v_and_b32 v227, s95, v95\n"
        "v_fma_f32 v226, v226, v14, v15\n"
        "v_mfma_f32_32x32x16_bf16 a[32:47], a[144:147], v[84:87], a[32:47]\n"
        "v_add_u32 v6, 65536, v6\n"
        "ds_read_b64_tr_b16 a[144:145], v6 offset:6144\n"
        "ds_read_b64_tr_b16 a[146:147], v6 offset:6656\n"
        "v_fma_f32 v227, v227, v14, v15\n"
        "v_cvt_pk_bf16_f32 v95, v226, v227\n"
        "v_lshlrev_b32 v224, 16, v96\n"
        "v_mfma_f32_32x32x16_bf16 a[48:63], a[148:151], v[84:87], a[48:63]\n"
        "v_add_u32 v7, 65536, v7\n"
        "ds_read_b64_tr_b16 a[148:149], v7 offset:6144\n"
        "ds_read_b64_tr_b16 a[150:151], v7 offset:6656\n"
        "v_and_b32 v225, s95, v96\n"
        "v_fma_f32 v224, v224, v14, v15\n"
        "v_fma_f32 v225, v225, v14, v15\n"
        "s_waitcnt lgkmcnt(10)\n"
        "v_mfma_f32_32x32x16_bf16 v[128:143], v[88:91], a[64:67], v[128:143]\n"
        "v_cvt_pk_bf16_f32 v96, v224, v225\n"
        "v_lshlrev_b32 v226, 16, v97\n"
        "v_and_b32 v227, s95, v97\n"
        "v_fma_f32 v226, v226, v14, v15\n"
        "v_fma_f32 v227, v227, v14, v15\n"
        "v_mfma_f32_32x32x16_bf16 v[144:159], v[88:91], a[68:71], v[144:159]\n"
        "v_cvt_pk_bf16_f32 v97, v226, v227\n"
        "v_lshlrev_b32 v224, 16, v98\n"
        "v_and_b32 v225, s95, v98\n"
        "v_fma_f32 v224, v224, v14, v15\n"
        "v_fma_f32 v225, v225, v14, v15\n"
        "v_cvt_pk_bf16_f32 v98, v224, v225\n"
        "v_mfma_f32_32x32x16_bf16 a[0:15], a[72:75], v[88:91], a[0:15]\n"
        "s_waitcnt vmcnt(24) lgkmcnt(0)\n"
        "s_barrier\n"
        "v_add_u32 v2, 65536, v2\n"
        "ds_read_b128 a[64:67], v2 offset:8192\n"
        "ds_read_b128 a[68:71], v2 offset:12288\n"
        "ds_read_b64_tr_b16 a[72:73], v6 offset:16384\n"
        "ds_read_b64_tr_b16 a[74:75], v6 offset:16896\n"
        "s_add_u32 m0, s72, 49152\n"
        "s_nop 0\n"
        "buffer_load_dwordx4 v8, s[48:51], s92 offen lds\n"
        "v_mfma_f32_32x32x16_bf16 a[16:31], a[76:79], v[88:91], a[16:31]\n"
        "ds_read_b64_tr_b16 a[76:77], v7 offset:16384\n"
        "ds_read_b64_tr_b16 a[78:79], v7 offset:16896\n"
        "buffer_load_dwordx4 v9, s[48:51], s92 offen offset:1024 lds\n"
        "v_lshlrev_b32 v226, 16, v99\n"
        "v_and_b32 v227, s95, v99\n"
        "v_fma_f32 v226, v226, v14, v15\n"
        "v_mfma_f32_32x32x16_bf16 a[32:47], a[80:83], v[88:91], a[32:47]\n"
        "ds_read_b64_tr_b16 a[80:81], v6 offset:24576\n"
        "ds_read_b64_tr_b16 a[82:83], v6 offset:25088\n"
        "v_fma_f32 v227, v227, v14, v15\n"
        "v_cvt_pk_bf16_f32 v99, v226, v227\n"
        "v_lshlrev_b32 v224, 16, v100\n"
        "v_and_b32 v225, s95, v100\n"
        "v_mfma_f32_32x32x16_bf16 a[48:63], a[84:87], v[88:91], a[48:63]\n"
        "ds_read_b64_tr_b16 a[84:85], v7 offset:24576\n"
        "ds_read_b64_tr_b16 a[86:87], v7 offset:25088\n"
        "buffer_load_dwordx4 v10, s[48:51], s92 offen offset:2048 lds\n"
        "v_fma_f32 v224, v224, v14, v15\n"
        "v_fma_f32 v225, v225, v14, v15\n"
        "v_cvt_pk_bf16_f32 v100, v224, v225\n"
        "v_mfma_f32_32x32x16_bf16 v[128:143], v[92:95], a[128:131], v[128:143]\n"
        "v_add_u32 v3, 65536, v3\n"
        "ds_read_b128 a[128:131], v3 offset:8192\n"
        "v_lshlrev_b32 v226, 16, v101\n"
        "v_and_b32 v227, s95, v101\n"
        "v_fma_f32 v226, v226, v14, v15\n"
        "v_fma_f32 v227, v227, v14, v15\n"
        "v_mfma_f32_32x32x16_bf16 v[144:159], v[92:95], a[132:135], v[144:159]\n"
        "ds_read_b128 a[132:135], v3 offset:12288\n"
        "buffer_load_dwordx4 v11, s[48:51], s92 offen offset:3072 lds\n"
        "v_cvt_pk_bf16_f32 v101, v226, v227\n"
        "v_lshlrev_b32 v224, 16, v102\n"
        "v_and_b32 v225, s95, v102\n"
        "v_fma_f32 v224, v224, v14, v15\n"
        "v_bfe_u32 v250, v252, 4, 2\n"
        "v_xor_b32 v250, 2, v250\n"
        "v_lshl_add_u32 v250, v250, 28, v248\n"
        "v_mfma_f32_32x32x16_bf16 a[0:15], a[136:139], v[92:95], a[0:15]\n"
        "ds_read_b64_tr_b16 a[136:137], v6 offset:18432\n"
        "ds_read_b64_tr_b16 a[138:139], v6 offset:18944\n"
        "v_fma_f32 v225, v225, v14, v15\n"
        "v_cvt_pk_bf16_f32 v102, v224, v225\n"
        "v_lshlrev_b32 v226, 16, v103\n"
        "v_and_b32 v227, s95, v103\n"
        "v_mfma_f32_32x32x16_bf16 a[16:31], a[140:143], v[92:95], a[16:31]\n"
        "ds_read_b64_tr_b16 a[140:141], v7 offset:18432\n"
        "ds_read_b64_tr_b16 a[142:143], v7 offset:18944\n"
        "buffer_load_dword v251, v250, s[52:55], 0 offen\n"
        "v_fma_f32 v226, v226, v14, v15\n"
        "v_fma_f32 v227, v227, v14, v15\n"
        "v_cvt_pk_bf16_f32 v103, v226, v227\n"
        "v_bfe_u32 v250, v252, 4, 2\n"
        "v_xor_b32 v250, 3, v250\n"
        "v_lshl_add_u32 v250, v250, 28, v248\n"
        "v_mfma_f32_32x32x16_bf16 a[32:47], a[144:147], v[92:95], a[32:47]\n"
        "ds_read_b64_tr_b16 a[144:145], v6 offset:26624\n"
        "ds_read_b64_tr_b16 a[146:147], v6 offset:27136\n"
        "v_lshlrev_b32 v224, 16, v104\n"
        "v_and_b32 v225, s95, v104\n"
        "v_fma_f32 v224, v224, v14, v15\n"
        "v_fma_f32 v225, v225, v14, v15\n"
        "v_mfma_f32_32x32x16_bf16 a[48:63], a[148:151], v[92:95], a[48:63]\n"
        "ds_read_b64_tr_b16 a[148:149], v7 offset:26624\n"
        "ds_read_b64_tr_b16 a[150:151], v7 offset:27136\n"
        "buffer_load_dword v251, v250, s[52:55], 0 offen\n"
        "v_cvt_pk_bf16_f32 v104, v224, v225\n"
        "v_lshlrev_b32 v226, 16, v105\n"
        "v_and_b32 v227, s95, v105\n"
        "s_waitcnt lgkmcnt(10)\n"
        "v_mfma_f32_32x32x16_bf16 v[128:143], v[96:99], a[64:67], v[128:143]\n"
        "v_add_u32 v4, 65536, v4\n"
        "ds_read_b128 a[64:67], v4 offset:8192\n"
        "v_fma_f32 v226, v226, v14, v15\n"
        "v_fma_f32 v227, v227, v14, v15\n"
        "v_cvt_pk_bf16_f32 v105, v226, v227\n"
        "v_mfma_f32_32x32x16_bf16 v[144:159], v[96:99], a[68:71], v[144:159]\n"
        "ds_read_b128 a[68:71], v4 offset:12288\n"
        "v_lshlrev_b32 v224, 16, v106\n"
        "v_and_b32 v225, s95, v106\n"
        "v_fma_f32 v224, v224, v14, v15\n"
        "v_fma_f32 v225, v225, v14, v15\n"
        "v_cvt_pk_bf16_f32 v106, v224, v225\n"
        "v_mfma_f32_32x32x16_bf16 a[0:15], a[72:75], v[96:99], a[0:15]\n"
        "ds_read_b64_tr_b16 a[72:73], v6 offset:20480\n"
        "ds_read_b64_tr_b16 a[74:75], v6 offset:20992\n"
        "v_lshlrev_b32 v226, 16, v107\n"
        "v_and_b32 v227, s95, v107\n"
        "v_fma_f32 v226, v226, v14, v15\n"
        "v_fma_f32 v227, v227, v14, v15\n"
        "v_mfma_f32_32x32x16_bf16 a[16:31], a[76:79], v[96:99], a[16:31]\n"
        "ds_read_b64_tr_b16 a[76:77], v7 offset:20480\n"
        "ds_read_b64_tr_b16 a[78:79], v7 offset:20992\n"
        "v_cvt_pk_bf16_f32 v107, v226, v227\n"
        "v_lshlrev_b32 v224, 16, v108\n"
        "v_and_b32 v225, s95, v108\n"
        "v_fma_f32 v224, v224, v14, v15\n"
        "v_mfma_f32_32x32x16_bf16 a[32:47], a[80:83], v[96:99], a[32:47]\n"
        "ds_read_b64_tr_b16 a[80:81], v6 offset:28672\n"
        "ds_read_b64_tr_b16 a[82:83], v6 offset:29184\n"
        "v_fma_f32 v225, v225, v14, v15\n"
        "v_cvt_pk_bf16_f32 v108, v224, v225\n"
        "v_lshlrev_b32 v226, 16, v109\n"
        "v_and_b32 v227, s95, v109\n"
        "v_mfma_f32_32x32x16_bf16 a[48:63], a[84:87], v[96:99], a[48:63]\n"
        "ds_read_b64_tr_b16 a[84:85], v7 offset:28672\n"
        "ds_read_b64_tr_b16 a[86:87], v7 offset:29184\n"
        "v_fma_f32 v226, v226, v14, v15\n"
        "v_fma_f32 v227, v227, v14, v15\n"
        "v_cvt_pk_bf16_f32 v109, v226, v227\n"
        "v_lshlrev_b32 v224, 16, v110\n"
        "s_waitcnt lgkmcnt(10)\n"
        "v_mfma_f32_32x32x16_bf16 v[128:143], v[100:103], a[128:131], v[128:143]\n"
        "v_add_u32 v5, 65536, v5\n"
        "ds_read_b128 a[128:131], v5 offset:8192\n"
        "v_and_b32 v225, s95, v110\n"
        "v_fma_f32 v224, v224, v14, v15\n"
        "v_fma_f32 v225, v225, v14, v15\n"
        "v_mfma_f32_32x32x16_bf16 v[144:159], v[100:103], a[132:135], v[144:159]\n"
        "ds_read_b128 a[132:135], v5 offset:12288\n"
        "v_cvt_pk_bf16_f32 v110, v224, v225\n"
        "v_lshlrev_b32 v226, 16, v111\n"
        "v_and_b32 v227, s95, v111\n"
        "v_fma_f32 v226, v226, v14, v15\n"
        "v_fma_f32 v227, v227, v14, v15\n"
        "v_mfma_f32_32x32x16_bf16 a[0:15], a[136:139], v[100:103], a[0:15]\n"
        "ds_read_b64_tr_b16 a[136:137], v6 offset:22528\n"
        "ds_read_b64_tr_b16 a[138:139], v6 offset:23040\n"
        "v_cvt_pk_bf16_f32 v111, v226, v227\n"
        "v_lshlrev_b32 v224, 16, v112\n"
        "v_and_b32 v225, s95, v112\n"
        "v_fma_f32 v224, v224, v14, v15\n"
        "v_mfma_f32_32x32x16_bf16 a[16:31], a[140:143], v[100:103], a[16:31]\n"
        "ds_read_b64_tr_b16 a[140:141], v7 offset:22528\n"
        "ds_read_b64_tr_b16 a[142:143], v7 offset:23040\n"
        "v_fma_f32 v225, v225, v14, v15\n"
        "v_cvt_pk_bf16_f32 v112, v224, v225\n"
        "v_lshlrev_b32 v226, 16, v113\n"
        "v_and_b32 v227, s95, v113\n"
        "v_mfma_f32_32x32x16_bf16 a[32:47], a[144:147], v[100:103], a[32:47]\n"
        "ds_read_b64_tr_b16 a[144:145], v6 offset:30720\n"
        "ds_read_b64_tr_b16 a[146:147], v6 offset:31232\n"
        "v_fma_f32 v226, v226, v14, v15\n"
        "v_fma_f32 v227, v227, v14, v15\n"
        "v_cvt_pk_bf16_f32 v113, v226, v227\n"
        "v_lshlrev_b32 v224, 16, v114\n"
        "v_mfma_f32_32x32x16_bf16 a[48:63], a[148:151], v[100:103], a[48:63]\n"
        "ds_read_b64_tr_b16 a[148:149], v7 offset:30720\n"
        "ds_read_b64_tr_b16 a[150:151], v7 offset:31232\n"
        "v_and_b32 v225, s95, v114\n"
        "v_fma_f32 v224, v224, v14, v15\n"
        "v_fma_f32 v225, v225, v14, v15\n"
        "v_cvt_pk_bf16_f32 v114, v224, v225\n"
        "s_waitcnt lgkmcnt(10)\n"
        "v_mfma_f32_32x32x16_bf16 v[128:143], v[104:107], a[64:67], v[128:143]\n"
        "v_lshlrev_b32 v226, 16, v115\n"
        "v_and_b32 v227, s95, v115\n"
        "v_fma_f32 v226, v226, v14, v15\n"
        "v_fma_f32 v227, v227, v14, v15\n"
        "v_cvt_pk_bf16_f32 v115, v226, v227\n"
        "v_mfma_f32_32x32x16_bf16 v[144:159], v[104:107], a[68:71], v[144:159]\n"
        "v_lshlrev_b32 v224, 16, v116\n"
        "v_and_b32 v225, s95, v116\n"
        "v_fma_f32 v224, v224, v14, v15\n"
        "v_fma_f32 v225, v225, v14, v15\n"
        "v_cvt_pk_bf16_f32 v116, v224, v225\n"
        "v_lshlrev_b32 v226, 16, v117\n"
        "v_mfma_f32_32x32x16_bf16 a[0:15], a[72:75], v[104:107], a[0:15]\n"
        "s_waitcnt vmcnt(24) lgkmcnt(0)\n"
        "s_barrier\n"
        "ds_read_b128 a[64:67], v2 offset:32768\n"
        "ds_read_b128 a[68:71], v2 offset:36864\n"
        "ds_read_b64_tr_b16 a[72:73], v6 offset:40960\n"
        "ds_read_b64_tr_b16 a[74:75], v6 offset:41472\n"
        "s_add_u32 m0, s72, 65536\n"
        "s_nop 0\n"
        "buffer_load_dwordx4 v8, s[48:51], s93 offen lds\n"
        "v_mfma_f32_32x32x16_bf16 a[16:31], a[76:79], v[104:107], a[16:31]\n"
        "ds_read_b64_tr_b16 a[76:77], v7 offset:40960\n"
        "ds_read_b64_tr_b16 a[78:79], v7 offset:41472\n"
        "buffer_load_dwordx4 v9, s[48:51], s93 offen offset:1024 lds\n"
        "v_and_b32 v227, s95, v117\n"
        "v_fma_f32 v226, v226, v14, v15\n"
        "v_fma_f32 v227, v227, v14, v15\n"
        "v_mfma_f32_32x32x16_bf16 a[32:47], a[80:83], v[104:107], a[32:47]\n"
        "ds_read_b64_tr_b16 a[80:81], v6 offset:49152\n"
        "ds_read_b64_tr_b16 a[82:83], v6 offset:49664\n"
        "v_cvt_pk_bf16_f32 v117, v226, v227\n"
        "v_lshlrev_b32 v224, 16, v118\n"
        "v_and_b32 v225, s95, v118\n"
        "v_fma_f32 v224, v224, v14, v15\n"
        "v_mfma_f32_32x32x16_bf16 a[48:63], a[84:87], v[104:107], a[48:63]\n"
        "ds_read_b64_tr_b16 a[84:85], v7 offset:49152\n"
        "ds_read_b64_tr_b16 a[86:87], v7 offset:49664\n"
        "buffer_load_dwordx4 v10, s[48:51], s93 offen offset:2048 lds\n"
        "v_fma_f32 v225, v225, v14, v15\n"
        "v_cvt_pk_bf16_f32 v118, v224, v225\n"
        "v_lshlrev_b32 v226, 16, v119\n"
        "v_mfma_f32_32x32x16_bf16 v[128:143], v[108:111], a[128:131], v[128:143]\n"
        "ds_read_b128 a[128:131], v3 offset:32768\n"
        "v_and_b32 v227, s95, v119\n"
        "v_fma_f32 v226, v226, v14, v15\n"
        "v_fma_f32 v227, v227, v14, v15\n"
        "v_cvt_pk_bf16_f32 v119, v226, v227\n"
        "v_lshlrev_b32 v224, 16, v120\n"
        "v_mfma_f32_32x32x16_bf16 v[144:159], v[108:111], a[132:135], v[144:159]\n"
        "ds_read_b128 a[132:135], v3 offset:36864\n"
        "buffer_load_dwordx4 v11, s[48:51], s93 offen offset:3072 lds\n"
        "v_and_b32 v225, s95, v120\n"
        "v_fma_f32 v224, v224, v14, v15\n"
        "v_fma_f32 v225, v225, v14, v15\n"
        "v_cvt_pk_bf16_f32 v120, v224, v225\n"
        "s_add_u32 m0, s72, 81920\n"
        "v_mfma_f32_32x32x16_bf16 a[0:15], a[136:139], v[108:111], a[0:15]\n"
        "ds_read_b64_tr_b16 a[136:137], v6 offset:43008\n"
        "ds_read_b64_tr_b16 a[138:139], v6 offset:43520\n"
        "v_lshlrev_b32 v226, 16, v121\n"
        "v_and_b32 v227, s95, v121\n"
        "v_fma_f32 v226, v226, v14, v15\n"
        "v_fma_f32 v227, v227, v14, v15\n"
        "v_mfma_f32_32x32x16_bf16 a[16:31], a[140:143], v[108:111], a[16:31]\n"
        "ds_read_b64_tr_b16 a[140:141], v7 offset:43008\n"
        "ds_read_b64_tr_b16 a[142:143], v7 offset:43520\n"
        "buffer_load_dwordx4 v8, s[48:51], s94 offen lds\n"
        "v_cvt_pk_bf16_f32 v121, v226, v227\n"
        "v_lshlrev_b32 v224, 16, v122\n"
        "v_and_b32 v225, s95, v122\n"
        "v_mfma_f32_32x32x16_bf16 a[32:47], a[144:147], v[108:111], a[32:47]\n"
        "ds_read_b64_tr_b16 a[144:145], v6 offset:51200\n"
        "ds_read_b64_tr_b16 a[146:147], v6 offset:51712\n"
        "v_fma_f32 v224, v224, v14, v15\n"
        "v_fma_f32 v225, v225, v14, v15\n"
        "v_cvt_pk_bf16_f32 v122, v224, v225\n"
        "v_lshlrev_b32 v226, 16, v123\n"
        "v_mfma_f32_32x32x16_bf16 a[48:63], a[148:151], v[108:111], a[48:63]\n"
        "ds_read_b64_tr_b16 a[148:149], v7 offset:51200\n"
        "ds_read_b64_tr_b16 a[150:151], v7 offset:51712\n"
        "buffer_load_dwordx4 v9, s[48:51], s94 offen offset:1024 lds\n"
        "v_and_b32 v227, s95, v123\n"
        "v_fma_f32 v226, v226, v14, v15\n"
        "v_fma_f32 v227, v227, v14, v15\n"
        "s_waitcnt lgkmcnt(10)\n"
        "v_mfma_f32_32x32x16_bf16 v[128:143], v[112:115], a[64:67], v[128:143]\n"
        "ds_read_b128 a[64:67], v4 offset:32768\n"
        "v_cvt_pk_bf16_f32 v123, v226, v227\n"
        "v_lshlrev_b32 v224, 16, v124\n"
        "v_and_b32 v225, s95, v124\n"
        "v_fma_f32 v224, v224, v14, v15\n"
        "v_mfma_f32_32x32x16_bf16 v[144:159], v[112:115], a[68:71], v[144:159]\n"
        "ds_read_b128 a[68:71], v4 offset:36864\n"
        "buffer_load_dwordx4 v10, s[48:51], s94 offen offset:2048 lds\n"
        "v_fma_f32 v225, v225, v14, v15\n"
        "v_cvt_pk_bf16_f32 v124, v224, v225\n"
        "v_lshlrev_b32 v226, 16, v125\n"
        "v_and_b32 v227, s95, v125\n"
        "v_mfma_f32_32x32x16_bf16 a[0:15], a[72:75], v[112:115], a[0:15]\n"
        "ds_read_b64_tr_b16 a[72:73], v6 offset:45056\n"
        "ds_read_b64_tr_b16 a[74:75], v6 offset:45568\n"
        "v_fma_f32 v226, v226, v14, v15\n"
        "v_fma_f32 v227, v227, v14, v15\n"
        "v_cvt_pk_bf16_f32 v125, v226, v227\n"
        "v_lshlrev_b32 v224, 16, v126\n"
        "v_mfma_f32_32x32x16_bf16 a[16:31], a[76:79], v[112:115], a[16:31]\n"
        "ds_read_b64_tr_b16 a[76:77], v7 offset:45056\n"
        "ds_read_b64_tr_b16 a[78:79], v7 offset:45568\n"
        "buffer_load_dwordx4 v11, s[48:51], s94 offen offset:3072 lds\n"
        "v_and_b32 v225, s95, v126\n"
        "v_fma_f32 v224, v224, v14, v15\n"
        "v_fma_f32 v225, v225, v14, v15\n"
        "v_mfma_f32_32x32x16_bf16 a[32:47], a[80:83], v[112:115], a[32:47]\n"
        "ds_read_b64_tr_b16 a[80:81], v6 offset:53248\n"
        "ds_read_b64_tr_b16 a[82:83], v6 offset:53760\n"
        "v_cvt_pk_bf16_f32 v126, v224, v225\n"
        "v_lshlrev_b32 v226, 16, v127\n"
        "v_and_b32 v227, s95, v127\n"
        "v_fma_f32 v226, v226, v14, v15\n"
        "v_mfma_f32_32x32x16_bf16 a[48:63], a[84:87], v[112:115], a[48:63]\n"
        "ds_read_b64_tr_b16 a[84:85], v7 offset:53248\n"
        "ds_read_b64_tr_b16 a[86:87], v7 offset:53760\n"
        "v_fma_f32 v227, v227, v14, v15\n"
        "v_cvt_pk_bf16_f32 v127, v226, v227\n"
        "s_waitcnt lgkmcnt(10)\n"
        "v_mfma_f32_32x32x16_bf16 v[128:143], v[116:119], a[128:131], v[128:143]\n"
        "ds_read_b128 a[128:131], v5 offset:32768\n"
        "v_mfma_f32_32x32x16_bf16 v[144:159], v[116:119], a[132:135], v[144:159]\n"
        "ds_read_b128 a[132:135], v5 offset:36864\n"
        "v_mfma_f32_32x32x16_bf16 a[0:15], a[136:139], v[116:119], a[0:15]\n"
        "ds_read_b64_tr_b16 a[136:137], v6 offset:47104\n"
        "ds_read_b64_tr_b16 a[138:139], v6 offset:47616\n"
        "v_mfma_f32_32x32x16_bf16 a[16:31], a[140:143], v[116:119], a[16:31]\n"
        "ds_read_b64_tr_b16 a[140:141], v7 offset:47104\n"
        "ds_read_b64_tr_b16 a[142:143], v7 offset:47616\n"
        "v_mfma_f32_32x32x16_bf16 a[32:47], a[144:147], v[116:119], a[32:47]\n"
        "ds_read_b64_tr_b16 a[144:145], v6 offset:55296\n"
        "ds_read_b64_tr_b16 a[146:147], v6 offset:55808\n"
        "v_mfma_f32_32x32x16_bf16 a[48:63], a[148:151], v[116:119], a[48:63]\n"
        "ds_read_b64_tr_b16 a[148:149], v7 offset:55296\n"
        "ds_read_b64_tr_b16 a[150:151], v7 offset:55808\n"
        "s_waitcnt lgkmcnt(10)\n"
        "v_mfma_f32_32x32x16_bf16 v[128:143], v[120:123], a[64:67], v[128:143]\n"
        "v_mfma_f32_32x32x16_bf16 v[144:159], v[120:123], a[68:71], v[144:159]\n"
        "v_mfma_f32_32x32x16_bf16 a[0:15], a[72:75], v[120:123], a[0:15]\n"
        "s_waitcnt vmcnt(28) lgkmcnt(0)\n"
        "s_barrier\n"
        "v_add_u32 v6, 65536, v6\n"
        "ds_read_b64_tr_b16 a[72:73], v6\n"
        "ds_read_b64_tr_b16 a[74:75], v6 offset:512\n"
        "v_accvgpr_read_b32 v8, a90\n"
        "v_add_u32 v9, 6144, v8\n"
        "v_add_u32 v10, 12288, v8\n"
        "v_add_u32 v11, 18432, v8\n"
        "s_add_u32 m0, s72, 98304\n"
        "s_nop 0\n"
        "buffer_load_dwordx4 v8, s[52:55], s8 offen lds\n"
        "v_mfma_f32_32x32x16_bf16 a[16:31], a[76:79], v[120:123], a[16:31]\n"
        "v_add_u32 v7, 65536, v7\n"
        "ds_read_b64_tr_b16 a[76:77], v7\n"
        "ds_read_b64_tr_b16 a[78:79], v7 offset:512\n"
        "buffer_load_dwordx4 v9, s[52:55], s8 offen offset:1024 lds\n"
        "v_mfma_f32_32x32x16_bf16 a[32:47], a[80:83], v[120:123], a[32:47]\n"
        "ds_read_b64_tr_b16 a[80:81], v6 offset:8192\n"
        "ds_read_b64_tr_b16 a[82:83], v6 offset:8704\n"
        "v_mfma_f32_32x32x16_bf16 a[48:63], a[84:87], v[120:123], a[48:63]\n"
        "ds_read_b64_tr_b16 a[84:85], v7 offset:8192\n"
        "ds_read_b64_tr_b16 a[86:87], v7 offset:8704\n"
        "buffer_load_dwordx4 v10, s[52:55], s8 offen offset:2048 lds\n"
        "v_mfma_f32_32x32x16_bf16 v[128:143], v[124:127], a[128:131], v[128:143]\n"
        "v_mfma_f32_32x32x16_bf16 v[144:159], v[124:127], a[132:135], v[144:159]\n"
        "buffer_load_dwordx4 v11, s[52:55], s8 offen offset:3072 lds\n"
        "s_add_u32 m0, s72, 114688\n"
        "v_mfma_f32_32x32x16_bf16 a[0:15], a[136:139], v[124:127], a[0:15]\n"
        "ds_read_b64_tr_b16 a[240:241], v6 offset:2048\n"
        "ds_read_b64_tr_b16 a[242:243], v6 offset:2560\n"
        "v_mfma_f32_32x32x16_bf16 a[16:31], a[140:143], v[124:127], a[16:31]\n"
        "ds_read_b64_tr_b16 a[244:245], v7 offset:2048\n"
        "ds_read_b64_tr_b16 a[246:247], v7 offset:2560\n"
        "buffer_load_dwordx4 v8, s[52:55], s9 offen lds\n"
        "v_mfma_f32_32x32x16_bf16 a[32:47], a[144:147], v[124:127], a[32:47]\n"
        "ds_read_b64_tr_b16 a[248:249], v6 offset:10240\n"
        "ds_read_b64_tr_b16 a[250:251], v6 offset:10752\n"
        "v_mfma_f32_32x32x16_bf16 a[48:63], a[148:151], v[124:127], a[48:63]\n"
        "ds_read_b64_tr_b16 a[252:253], v7 offset:10240\n"
        "ds_read_b64_tr_b16 a[254:255], v7 offset:10752\n"
        "buffer_load_dwordx4 v9, s[52:55], s9 offen offset:1024 lds\n"
        "s_waitcnt lgkmcnt(8)\n"
        "v_mfma_f32_32x32x16_bf16 v[160:175], a[72:75], v[16:19], 0\n"
        "ds_read_b64_tr_b16 a[72:73], v6 offset:4096\n"
        "ds_read_b64_tr_b16 a[74:75], v6 offset:4608\n"
        "v_mov_b32 v14, s96\n"
        "v_mov_b32 v15, s97\n"
        "v_accvgpr_read_b32 v229, a95\n"
        "v_mfma_f32_32x32x16_bf16 v[176:191], a[76:79], v[16:19], 0\n"
        "ds_read_b64_tr_b16 a[76:77], v7 offset:4096\n"
        "ds_read_b64_tr_b16 a[78:79], v7 offset:4608\n"
        "buffer_load_dwordx4 v10, s[52:55], s9 offen offset:2048 lds\n"
        "v_accvgpr_read_b32 v230, a0\n"
        "v_accvgpr_read_b32 v231, a32\n"
        "v_accvgpr_read_b32 v232, a1\n"
        "v_mfma_f32_32x32x16_bf16 v[192:207], a[80:83], v[16:19], 0\n"
        "ds_read_b64_tr_b16 a[80:81], v6 offset:12288\n"
        "ds_read_b64_tr_b16 a[82:83], v6 offset:12800\n"
        "buffer_load_dwordx4 v11, s[52:55], s9 offen offset:3072 lds\n"
        "v_accvgpr_read_b32 v233, a33\n"
        "v_max_f32 v230, 0, v230\n"
        "v_max_f32 v232, 0, v232\n"
        "v_mfma_f32_32x32x16_bf16 v[208:223], a[84:87], v[16:19], 0\n"
        "ds_read_b64_tr_b16 a[84:85], v7 offset:12288\n"
        "ds_read_b64_tr_b16 a[86:87], v7 offset:12800\n"
        "v_mul_f32 v230, v230, v231\n"
        "v_mul_f32 v232, v232, v233\n"
        "v_cvt_pk_bf16_f32 v238, v230, v232\n"
        "v_accvgpr_read_b32 v234, a2\n"
        "s_waitcnt lgkmcnt(8)\n"
        "v_mfma_f32_32x32x16_bf16 v[160:175], a[240:243], v[20:23], v[160:175]\n"
        "ds_read_b64_tr_b16 a[240:241], v6 offset:6144\n"
        "ds_read_b64_tr_b16 a[242:243], v6 offset:6656\n"
        "v_accvgpr_read_b32 v235, a34\n"
        "v_accvgpr_read_b32 v236, a3\n"
        "v_accvgpr_read_b32 v237, a35\n"
        "v_mfma_f32_32x32x16_bf16 v[176:191], a[244:247], v[20:23], v[176:191]\n"
        "ds_read_b64_tr_b16 a[244:245], v7 offset:6144\n"
        "ds_read_b64_tr_b16 a[246:247], v7 offset:6656\n"
        "v_max_f32 v234, 0, v234\n"
        "v_max_f32 v236, 0, v236\n"
        "v_mul_f32 v234, v234, v235\n"
        "v_mul_f32 v236, v236, v237\n"
        "v_mfma_f32_32x32x16_bf16 v[192:207], a[248:251], v[20:23], v[192:207]\n"
        "ds_read_b64_tr_b16 a[248:249], v6 offset:14336\n"
        "ds_read_b64_tr_b16 a[250:251], v6 offset:14848\n"
        "v_cvt_pk_bf16_f32 v239, v234, v236\n"
        "v_accvgpr_read_b32 v230, a4\n"
        "v_accvgpr_read_b32 v231, a36\n"
        "v_accvgpr_read_b32 v232, a5\n"
        "v_mfma_f32_32x32x16_bf16 v[208:223], a[252:255], v[20:23], v[208:223]\n"
        "ds_read_b64_tr_b16 a[252:253], v7 offset:14336\n"
        "ds_read_b64_tr_b16 a[254:255], v7 offset:14848\n"
        "v_accvgpr_read_b32 v233, a37\n"
        "v_max_f32 v230, 0, v230\n"
        "v_max_f32 v232, 0, v232\n"
        "v_mul_f32 v230, v230, v231\n"
        "s_waitcnt lgkmcnt(8)\n"
        "v_mfma_f32_32x32x16_bf16 v[160:175], a[72:75], v[24:27], v[160:175]\n"
        "v_mul_f32 v232, v232, v233\n"
        "v_cvt_pk_bf16_f32 v240, v230, v232\n"
        "v_accvgpr_read_b32 v234, a6\n"
        "v_accvgpr_read_b32 v235, a38\n"
        "v_accvgpr_read_b32 v236, a7\n"
        "v_mfma_f32_32x32x16_bf16 v[176:191], a[76:79], v[24:27], v[176:191]\n"
        "s_waitcnt vmcnt(32) lgkmcnt(0)\n"
        "s_barrier\n"
        "v_add_u32 v6, 4294836224, v6\n"
        "ds_read_b64_tr_b16 a[72:73], v6\n"
        "ds_read_b64_tr_b16 a[74:75], v6 offset:512\n"
        "v_add_u32 v7, 4294836224, v7\n"
        "ds_read_b64_tr_b16 a[76:77], v7\n"
        "ds_read_b64_tr_b16 a[78:79], v7 offset:512\n"
        "s_add_u32 m0, s72, 131072\n"
        "s_nop 0\n"
        "buffer_load_dwordx4 v8, s[52:55], s10 offen lds\n"
        "v_mfma_f32_32x32x16_bf16 v[192:207], a[80:83], v[24:27], v[192:207]\n"
        "ds_read_b64_tr_b16 a[80:81], v6 offset:8192\n"
        "ds_read_b64_tr_b16 a[82:83], v6 offset:8704\n"
        "buffer_load_dwordx4 v9, s[52:55], s10 offen offset:1024 lds\n"
        "v_accvgpr_read_b32 v237, a39\n"
        "v_max_f32 v234, 0, v234\n"
        "v_max_f32 v236, 0, v236\n"
        "v_mfma_f32_32x32x16_bf16 v[208:223], a[84:87], v[24:27], v[208:223]\n"
        "ds_read_b64_tr_b16 a[84:85], v7 offset:8192\n"
        "ds_read_b64_tr_b16 a[86:87], v7 offset:8704\n"
        "v_mul_f32 v234, v234, v235\n"
        "v_mul_f32 v236, v236, v237\n"
        "v_cvt_pk_bf16_f32 v241, v234, v236\n"
        "buffer_store_dwordx4 v[238:241], v229, s[56:59], 0 offen sc1\n"
        "v_mfma_f32_32x32x16_bf16 v[160:175], a[240:243], v[28:31], v[160:175]\n"
        "ds_read_b64_tr_b16 a[240:241], v6 offset:2048\n"
        "ds_read_b64_tr_b16 a[242:243], v6 offset:2560\n"
        "v_accvgpr_read_b32 v230, a8\n"
        "v_accvgpr_read_b32 v231, a40\n"
        "v_accvgpr_read_b32 v232, a9\n"
        "v_accvgpr_read_b32 v233, a41\n"
        "v_mfma_f32_32x32x16_bf16 v[176:191], a[244:247], v[28:31], v[176:191]\n"
        "ds_read_b64_tr_b16 a[244:245], v7 offset:2048\n"
        "ds_read_b64_tr_b16 a[246:247], v7 offset:2560\n"
        "v_max_f32 v230, 0, v230\n"
        "v_max_f32 v232, 0, v232\n"
        "v_mul_f32 v230, v230, v231\n"
        "v_mul_f32 v232, v232, v233\n"
        "v_mfma_f32_32x32x16_bf16 v[192:207], a[248:251], v[28:31], v[192:207]\n"
        "ds_read_b64_tr_b16 a[248:249], v6 offset:10240\n"
        "ds_read_b64_tr_b16 a[250:251], v6 offset:10752\n"
        "buffer_load_dwordx4 v10, s[52:55], s10 offen offset:2048 lds\n"
        "v_cvt_pk_bf16_f32 v242, v230, v232\n"
        "v_accvgpr_read_b32 v234, a10\n"
        "v_accvgpr_read_b32 v235, a42\n"
        "v_mfma_f32_32x32x16_bf16 v[208:223], a[252:255], v[28:31], v[208:223]\n"
        "ds_read_b64_tr_b16 a[252:253], v7 offset:10240\n"
        "ds_read_b64_tr_b16 a[254:255], v7 offset:10752\n"
        "v_accvgpr_read_b32 v236, a11\n"
        "v_accvgpr_read_b32 v237, a43\n"
        "v_max_f32 v234, 0, v234\n"
        "v_max_f32 v236, 0, v236\n"
        "s_waitcnt lgkmcnt(8)\n"
        "v_mfma_f32_32x32x16_bf16 v[160:175], a[72:75], v[32:35], v[160:175]\n"
        "ds_read_b64_tr_b16 a[72:73], v6 offset:4096\n"
        "ds_read_b64_tr_b16 a[74:75], v6 offset:4608\n"
        "buffer_load_dwordx4 v11, s[52:55], s10 offen offset:3072 lds\n"
        "v_mul_f32 v234, v234, v235\n"
        "v_mul_f32 v236, v236, v237\n"
        "v_bfe_u32 v250, v252, 4, 2\n"
        "v_xor_b32 v250, 0, v250\n"
        "v_lshl_add_u32 v250, v250, 28, v249\n"
        "v_mfma_f32_32x32x16_bf16 v[176:191], a[76:79], v[32:35], v[176:191]\n"
        "ds_read_b64_tr_b16 a[76:77], v7 offset:4096\n"
        "ds_read_b64_tr_b16 a[78:79], v7 offset:4608\n"
        "buffer_load_dword v251, v250, s[44:47], 0 offen\n"
        "v_cvt_pk_bf16_f32 v243, v234, v236\n"
        "v_accvgpr_read_b32 v230, a12\n"
        "v_accvgpr_read_b32 v231, a44\n"
        "v_mfma_f32_32x32x16_bf16 v[192:207], a[80:83], v[32:35], v[192:207]\n"
        "ds_read_b64_tr_b16 a[80:81], v6 offset:12288\n"
        "ds_read_b64_tr_b16 a[82:83], v6 offset:12800\n"
        "v_accvgpr_read_b32 v232, a13\n"
        "v_accvgpr_read_b32 v233, a45\n"
        "v_max_f32 v230, 0, v230\n"
        "v_max_f32 v232, 0, v232\n"
        "v_mfma_f32_32x32x16_bf16 v[208:223], a[84:87], v[32:35], v[208:223]\n"
        "ds_read_b64_tr_b16 a[84:85], v7 offset:12288\n"
        "ds_read_b64_tr_b16 a[86:87], v7 offset:12800\n"
        "v_mul_f32 v230, v230, v231\n"
        "v_mul_f32 v232, v232, v233\n"
        "v_cvt_pk_bf16_f32 v244, v230, v232\n"
        "v_accvgpr_read_b32 v234, a14\n"
        "s_waitcnt lgkmcnt(8)\n"
        "v_mfma_f32_32x32x16_bf16 v[160:175], a[240:243], v[36:39], v[160:175]\n"
        "ds_read_b64_tr_b16 a[240:241], v6 offset:6144\n"
        "ds_read_b64_tr_b16 a[242:243], v6 offset:6656\n"
        "v_accvgpr_read_b32 v235, a46\n"
        "v_accvgpr_read_b32 v236, a15\n"
        "v_accvgpr_read_b32 v237, a47\n"
        "v_mfma_f32_32x32x16_bf16 v[176:191], a[244:247], v[36:39], v[176:191]\n"
        "ds_read_b64_tr_b16 a[244:245], v7 offset:6144\n"
        "ds_read_b64_tr_b16 a[246:247], v7 offset:6656\n"
        "v_max_f32 v234, 0, v234\n"
        "v_max_f32 v236, 0, v236\n"
        "v_mul_f32 v234, v234, v235\n"
        "v_mul_f32 v236, v236, v237\n"
        "v_mfma_f32_32x32x16_bf16 v[192:207], a[248:251], v[36:39], v[192:207]\n"
        "ds_read_b64_tr_b16 a[248:249], v6 offset:14336\n"
        "ds_read_b64_tr_b16 a[250:251], v6 offset:14848\n"
        "v_cvt_pk_bf16_f32 v245, v234, v236\n"
        "buffer_store_dwordx4 v[242:245], v229, s[56:59], 0 offen offset:1024 sc1\n"
        "v_accvgpr_read_b32 v230, a16\n"
        "v_accvgpr_read_b32 v231, a48\n"
        "v_mfma_f32_32x32x16_bf16 v[208:223], a[252:255], v[36:39], v[208:223]\n"
        "ds_read_b64_tr_b16 a[252:253], v7 offset:14336\n"
        "ds_read_b64_tr_b16 a[254:255], v7 offset:14848\n"
        "v_accvgpr_read_b32 v232, a17\n"
        "v_accvgpr_read_b32 v233, a49\n"
        "v_max_f32 v230, 0, v230\n"
        "v_max_f32 v232, 0, v232\n"
        "s_waitcnt lgkmcnt(8)\n"
        "v_mfma_f32_32x32x16_bf16 v[160:175], a[72:75], v[40:43], v[160:175]\n"
        "v_mul_f32 v230, v230, v231\n"
        "v_mul_f32 v232, v232, v233\n"
        "v_cvt_pk_bf16_f32 v238, v230, v232\n"
        "v_accvgpr_read_b32 v234, a18\n"
        "v_accvgpr_read_b32 v235, a50\n"
        "v_mfma_f32_32x32x16_bf16 v[176:191], a[76:79], v[40:43], v[176:191]\n"
        "s_waitcnt vmcnt(35) lgkmcnt(0)\n"
        "s_barrier\n"
        "ds_read_b64_tr_b16 a[72:73], v6 offset:16384\n"
        "ds_read_b64_tr_b16 a[74:75], v6 offset:16896\n"
        "ds_read_b64_tr_b16 a[76:77], v7 offset:16384\n"
        "ds_read_b64_tr_b16 a[78:79], v7 offset:16896\n"
        "s_add_u32 m0, s72, 0\n"
        "s_nop 0\n"
        "buffer_load_dwordx4 v8, s[52:55], s11 offen lds\n"
        "v_mfma_f32_32x32x16_bf16 v[192:207], a[80:83], v[40:43], v[192:207]\n"
        "ds_read_b64_tr_b16 a[80:81], v6 offset:24576\n"
        "ds_read_b64_tr_b16 a[82:83], v6 offset:25088\n"
        "buffer_load_dwordx4 v9, s[52:55], s11 offen offset:1024 lds\n"
        "v_accvgpr_read_b32 v236, a19\n"
        "v_accvgpr_read_b32 v237, a51\n"
        "v_max_f32 v234, 0, v234\n"
        "v_mfma_f32_32x32x16_bf16 v[208:223], a[84:87], v[40:43], v[208:223]\n"
        "ds_read_b64_tr_b16 a[84:85], v7 offset:24576\n"
        "ds_read_b64_tr_b16 a[86:87], v7 offset:25088\n"
        "v_max_f32 v236, 0, v236\n"
        "v_mul_f32 v234, v234, v235\n"
        "v_mul_f32 v236, v236, v237\n"
        "v_cvt_pk_bf16_f32 v239, v234, v236\n"
        "v_mfma_f32_32x32x16_bf16 v[160:175], a[240:243], v[44:47], v[160:175]\n"
        "ds_read_b64_tr_b16 a[240:241], v6 offset:18432\n"
        "ds_read_b64_tr_b16 a[242:243], v6 offset:18944\n"
        "buffer_load_dwordx4 v10, s[52:55], s11 offen offset:2048 lds\n"
        "v_accvgpr_read_b32 v230, a20\n"
        "v_accvgpr_read_b32 v231, a52\n"
        "v_accvgpr_read_b32 v232, a21\n"
        "v_mfma_f32_32x32x16_bf16 v[176:191], a[244:247], v[44:47], v[176:191]\n"
        "ds_read_b64_tr_b16 a[244:245], v7 offset:18432\n"
        "ds_read_b64_tr_b16 a[246:247], v7 offset:18944\n"
        "v_accvgpr_read_b32 v233, a53\n"
        "v_max_f32 v230, 0, v230\n"
        "v_max_f32 v232, 0, v232\n"
        "v_mul_f32 v230, v230, v231\n"
        "v_mfma_f32_32x32x16_bf16 v[192:207], a[248:251], v[44:47], v[192:207]\n"
        "ds_read_b64_tr_b16 a[248:249], v6 offset:26624\n"
        "ds_read_b64_tr_b16 a[250:251], v6 offset:27136\n"
        "buffer_load_dwordx4 v11, s[52:55], s11 offen offset:3072 lds\n"
        "v_mul_f32 v232, v232, v233\n"
        "v_cvt_pk_bf16_f32 v240, v230, v232\n"
        "v_accvgpr_read_b32 v234, a22\n"
        "v_bfe_u32 v250, v252, 4, 2\n"
        "v_xor_b32 v250, 1, v250\n"
        "v_lshl_add_u32 v250, v250, 28, v249\n"
        "v_mfma_f32_32x32x16_bf16 v[208:223], a[252:255], v[44:47], v[208:223]\n"
        "ds_read_b64_tr_b16 a[252:253], v7 offset:26624\n"
        "ds_read_b64_tr_b16 a[254:255], v7 offset:27136\n"
        "v_accvgpr_read_b32 v235, a54\n"
        "v_accvgpr_read_b32 v236, a23\n"
        "v_accvgpr_read_b32 v237, a55\n"
        "v_max_f32 v234, 0, v234\n"
        "s_waitcnt lgkmcnt(8)\n"
        "v_mfma_f32_32x32x16_bf16 v[160:175], a[72:75], v[48:51], v[160:175]\n"
        "ds_read_b64_tr_b16 a[72:73], v6 offset:20480\n"
        "ds_read_b64_tr_b16 a[74:75], v6 offset:20992\n"
        "buffer_load_dword v251, v250, s[44:47], 0 offen\n"
        "v_max_f32 v236, 0, v236\n"
        "v_mul_f32 v234, v234, v235\n"
        "v_mfma_f32_32x32x16_bf16 v[176:191], a[76:79], v[48:51], v[176:191]\n"
        "ds_read_b64_tr_b16 a[76:77], v7 offset:20480\n"
        "ds_read_b64_tr_b16 a[78:79], v7 offset:20992\n"
        "v_mul_f32 v236, v236, v237\n"
        "v_cvt_pk_bf16_f32 v241, v234, v236\n"
        "buffer_store_dwordx4 v[238:241], v229, s[56:59], 0 offen offset:2048 sc1\n"
        "v_accvgpr_read_b32 v230, a24\n"
        "v_mfma_f32_32x32x16_bf16 v[192:207], a[80:83], v[48:51], v[192:207]\n"
        "ds_read_b64_tr_b16 a[80:81], v6 offset:28672\n"
        "ds_read_b64_tr_b16 a[82:83], v6 offset:29184\n"
        "v_accvgpr_read_b32 v231, a56\n"
        "v_accvgpr_read_b32 v232, a25\n"
        "v_accvgpr_read_b32 v233, a57\n"
        "v_max_f32 v230, 0, v230\n"
        "v_mfma_f32_32x32x16_bf16 v[208:223], a[84:87], v[48:51], v[208:223]\n"
        "ds_read_b64_tr_b16 a[84:85], v7 offset:28672\n"
        "ds_read_b64_tr_b16 a[86:87], v7 offset:29184\n"
        "v_max_f32 v232, 0, v232\n"
        "v_mul_f32 v230, v230, v231\n"
        "v_mul_f32 v232, v232, v233\n"
        "v_cvt_pk_bf16_f32 v242, v230, v232\n"
        "s_waitcnt lgkmcnt(8)\n"
        "v_mfma_f32_32x32x16_bf16 v[160:175], a[240:243], v[52:55], v[160:175]\n"
        "ds_read_b64_tr_b16 a[240:241], v6 offset:22528\n"
        "ds_read_b64_tr_b16 a[242:243], v6 offset:23040\n"
        "v_accvgpr_read_b32 v234, a26\n"
        "v_accvgpr_read_b32 v235, a58\n"
        "v_accvgpr_read_b32 v236, a27\n"
        "v_mfma_f32_32x32x16_bf16 v[176:191], a[244:247], v[52:55], v[176:191]\n"
        "ds_read_b64_tr_b16 a[244:245], v7 offset:22528\n"
        "ds_read_b64_tr_b16 a[246:247], v7 offset:23040\n"
        "v_accvgpr_read_b32 v237, a59\n"
        "v_max_f32 v234, 0, v234\n"
        "v_max_f32 v236, 0, v236\n"
        "v_mul_f32 v234, v234, v235\n"
        "v_mfma_f32_32x32x16_bf16 v[192:207], a[248:251], v[52:55], v[192:207]\n"
        "ds_read_b64_tr_b16 a[248:249], v6 offset:30720\n"
        "ds_read_b64_tr_b16 a[250:251], v6 offset:31232\n"
        "v_mul_f32 v236, v236, v237\n"
        "v_cvt_pk_bf16_f32 v243, v234, v236\n"
        "v_accvgpr_read_b32 v230, a28\n"
        "v_accvgpr_read_b32 v231, a60\n"
        "v_mfma_f32_32x32x16_bf16 v[208:223], a[252:255], v[52:55], v[208:223]\n"
        "ds_read_b64_tr_b16 a[252:253], v7 offset:30720\n"
        "ds_read_b64_tr_b16 a[254:255], v7 offset:31232\n"
        "v_accvgpr_read_b32 v232, a29\n"
        "v_accvgpr_read_b32 v233, a61\n"
        "v_max_f32 v230, 0, v230\n"
        "v_max_f32 v232, 0, v232\n"
        "s_waitcnt lgkmcnt(8)\n"
        "v_mfma_f32_32x32x16_bf16 v[160:175], a[72:75], v[56:59], v[160:175]\n"
        "v_mul_f32 v230, v230, v231\n"
        "v_mul_f32 v232, v232, v233\n"
        "v_cvt_pk_bf16_f32 v244, v230, v232\n"
        "v_accvgpr_read_b32 v234, a30\n"
        "v_accvgpr_read_b32 v235, a62\n"
        "v_mfma_f32_32x32x16_bf16 v[176:191], a[76:79], v[56:59], v[176:191]\n"
        "s_waitcnt vmcnt(37) lgkmcnt(0)\n"
        "s_barrier\n"
        "ds_read_b64_tr_b16 a[72:73], v6 offset:32768\n"
        "ds_read_b64_tr_b16 a[74:75], v6 offset:33280\n"
        "ds_read_b64_tr_b16 a[76:77], v7 offset:32768\n"
        "ds_read_b64_tr_b16 a[78:79], v7 offset:33280\n"
        "s_add_u32 m0, s72, 16384\n"
        "s_nop 0\n"
        "buffer_load_dwordx4 v8, s[52:55], s12 offen lds\n"
        "v_mfma_f32_32x32x16_bf16 v[192:207], a[80:83], v[56:59], v[192:207]\n"
        "ds_read_b64_tr_b16 a[80:81], v6 offset:40960\n"
        "ds_read_b64_tr_b16 a[82:83], v6 offset:41472\n"
        "buffer_load_dwordx4 v9, s[52:55], s12 offen offset:1024 lds\n"
        "v_accvgpr_read_b32 v236, a31\n"
        "v_accvgpr_read_b32 v237, a63\n"
        "v_max_f32 v234, 0, v234\n"
        "v_mfma_f32_32x32x16_bf16 v[208:223], a[84:87], v[56:59], v[208:223]\n"
        "ds_read_b64_tr_b16 a[84:85], v7 offset:40960\n"
        "ds_read_b64_tr_b16 a[86:87], v7 offset:41472\n"
        "v_max_f32 v236, 0, v236\n"
        "v_mul_f32 v234, v234, v235\n"
        "v_mul_f32 v236, v236, v237\n"
        "v_cvt_pk_bf16_f32 v245, v234, v236\n"
        "v_mfma_f32_32x32x16_bf16 v[160:175], a[240:243], v[60:63], v[160:175]\n"
        "ds_read_b64_tr_b16 a[240:241], v6 offset:34816\n"
        "ds_read_b64_tr_b16 a[242:243], v6 offset:35328\n"
        "buffer_load_dwordx4 v10, s[52:55], s12 offen offset:2048 lds\n"
        "buffer_store_dwordx4 v[242:245], v229, s[56:59], 0 offen offset:3072 sc1\n"
        "v_accvgpr_read_b32 v246, a115\n"
        "v_accvgpr_read_b32 v247, a116\n"
        "v_mfma_f32_32x32x16_bf16 v[176:191], a[244:247], v[60:63], v[176:191]\n"
        "ds_read_b64_tr_b16 a[244:245], v7 offset:34816\n"
        "ds_read_b64_tr_b16 a[246:247], v7 offset:35328\n"
        "v_fma_f32 v224, v246, v128, v129\n"
        "v_fma_f32 v225, v246, v144, v145\n"
        "v_fma_f32 v224, -v247, v144, v224\n"
        "v_fma_f32 v225, v247, v128, v225\n"
        "v_mfma_f32_32x32x16_bf16 v[192:207], a[248:251], v[60:63], v[192:207]\n"
        "ds_read_b64_tr_b16 a[248:249], v6 offset:43008\n"
        "ds_read_b64_tr_b16 a[250:251], v6 offset:43520\n"
        "v_fma_f32 v226, v246, v224, v130\n"
        "v_fma_f32 v227, v246, v225, v146\n"
        "v_fma_f32 v226, -v247, v225, v226\n"
        "v_fma_f32 v227, v247, v224, v227\n"
        "v_mfma_f32_32x32x16_bf16 v[208:223], a[252:255], v[60:63], v[208:223]\n"
        "ds_read_b64_tr_b16 a[252:253], v7 offset:43008\n"
        "ds_read_b64_tr_b16 a[254:255], v7 offset:43520\n"
        "v_fma_f32 v224, v246, v226, v131\n"
        "v_fma_f32 v225, v246, v227, v147\n"
        "v_fma_f32 v224, -v247, v227, v224\n"
        "v_fma_f32 v225, v247, v226, v225\n"
        "s_waitcnt lgkmcnt(8)\n"
        "v_mfma_f32_32x32x16_bf16 v[160:175], a[72:75], v[64:67], v[160:175]\n"
        "ds_read_b64_tr_b16 a[72:73], v6 offset:36864\n"
        "ds_read_b64_tr_b16 a[74:75], v6 offset:37376\n"
        "buffer_load_dwordx4 v11, s[52:55], s12 offen offset:3072 lds\n"
        "v_fma_f32 v226, v246, v224, v132\n"
        "v_fma_f32 v227, v246, v225, v148\n"
        "v_mfma_f32_32x32x16_bf16 v[176:191], a[76:79], v[64:67], v[176:191]\n"
        "ds_read_b64_tr_b16 a[76:77], v7 offset:36864\n"
        "ds_read_b64_tr_b16 a[78:79], v7 offset:37376\n"
        "v_fma_f32 v226, -v247, v225, v226\n"
        "v_fma_f32 v227, v247, v224, v227\n"
        "v_fma_f32 v224, v246, v226, v133\n"
        "v_fma_f32 v225, v246, v227, v149\n"
        "v_mfma_f32_32x32x16_bf16 v[192:207], a[80:83], v[64:67], v[192:207]\n"
        "ds_read_b64_tr_b16 a[80:81], v6 offset:45056\n"
        "ds_read_b64_tr_b16 a[82:83], v6 offset:45568\n"
        "v_fma_f32 v224, -v247, v227, v224\n"
        "v_fma_f32 v225, v247, v226, v225\n"
        "v_fma_f32 v226, v246, v224, v134\n"
        "v_fma_f32 v227, v246, v225, v150\n"
        "v_mfma_f32_32x32x16_bf16 v[208:223], a[84:87], v[64:67], v[208:223]\n"
        "ds_read_b64_tr_b16 a[84:85], v7 offset:45056\n"
        "ds_read_b64_tr_b16 a[86:87], v7 offset:45568\n"
        "v_fma_f32 v226, -v247, v225, v226\n"
        "v_fma_f32 v227, v247, v224, v227\n"
        "v_fma_f32 v224, v246, v226, v135\n"
        "v_fma_f32 v225, v246, v227, v151\n"
        "s_waitcnt lgkmcnt(8)\n"
        "v_mfma_f32_32x32x16_bf16 v[160:175], a[240:243], v[68:71], v[160:175]\n"
        "ds_read_b64_tr_b16 a[240:241], v6 offset:38912\n"
        "ds_read_b64_tr_b16 a[242:243], v6 offset:39424\n"
        "v_fma_f32 v224, -v247, v227, v224\n"
        "v_fma_f32 v225, v247, v226, v225\n"
        "v_fma_f32 v226, v246, v224, v136\n"
        "v_mfma_f32_32x32x16_bf16 v[176:191], a[244:247], v[68:71], v[176:191]\n"
        "ds_read_b64_tr_b16 a[244:245], v7 offset:38912\n"
        "ds_read_b64_tr_b16 a[246:247], v7 offset:39424\n"
        "v_fma_f32 v227, v246, v225, v152\n"
        "v_fma_f32 v226, -v247, v225, v226\n"
        "v_fma_f32 v227, v247, v224, v227\n"
        "v_fma_f32 v224, v246, v226, v137\n"
        "v_mfma_f32_32x32x16_bf16 v[192:207], a[248:251], v[68:71], v[192:207]\n"
        "ds_read_b64_tr_b16 a[248:249], v6 offset:47104\n"
        "ds_read_b64_tr_b16 a[250:251], v6 offset:47616\n"
        "v_fma_f32 v225, v246, v227, v153\n"
        "v_fma_f32 v224, -v247, v227, v224\n"
        "v_fma_f32 v225, v247, v226, v225\n"
        "v_fma_f32 v226, v246, v224, v138\n"
        "v_mfma_f32_32x32x16_bf16 v[208:223], a[252:255], v[68:71], v[208:223]\n"
        "ds_read_b64_tr_b16 a[252:253], v7 offset:47104\n"
        "ds_read_b64_tr_b16 a[254:255], v7 offset:47616\n"
        "v_fma_f32 v227, v246, v225, v154\n"
        "v_fma_f32 v226, -v247, v225, v226\n"
        "v_fma_f32 v227, v247, v224, v227\n"
        "v_fma_f32 v224, v246, v226, v139\n"
        "s_waitcnt lgkmcnt(8)\n"
        "v_mfma_f32_32x32x16_bf16 v[160:175], a[72:75], v[72:75], v[160:175]\n"
        "v_fma_f32 v225, v246, v227, v155\n"
        "v_fma_f32 v224, -v247, v227, v224\n"
        "v_fma_f32 v225, v247, v226, v225\n"
        "v_fma_f32 v226, v246, v224, v140\n"
        "v_fma_f32 v227, v246, v225, v156\n"
        "v_mfma_f32_32x32x16_bf16 v[176:191], a[76:79], v[72:75], v[176:191]\n"
        "s_waitcnt vmcnt(36) lgkmcnt(0)\n"
        "s_barrier\n"
        "ds_read_b64_tr_b16 a[72:73], v6 offset:49152\n"
        "ds_read_b64_tr_b16 a[74:75], v6 offset:49664\n"
        "ds_read_b64_tr_b16 a[76:77], v7 offset:49152\n"
        "ds_read_b64_tr_b16 a[78:79], v7 offset:49664\n"
        "s_add_u32 m0, s72, 32768\n"
        "s_nop 0\n"
        "buffer_load_dwordx4 v8, s[52:55], s13 offen lds\n"
        "v_mfma_f32_32x32x16_bf16 v[192:207], a[80:83], v[72:75], v[192:207]\n"
        "ds_read_b64_tr_b16 a[80:81], v6 offset:57344\n"
        "ds_read_b64_tr_b16 a[82:83], v6 offset:57856\n"
        "buffer_load_dwordx4 v9, s[52:55], s13 offen offset:1024 lds\n"
        "v_fma_f32 v226, -v247, v225, v226\n"
        "v_fma_f32 v227, v247, v224, v227\n"
        "v_fma_f32 v224, v246, v226, v141\n"
        "v_mfma_f32_32x32x16_bf16 v[208:223], a[84:87], v[72:75], v[208:223]\n"
        "ds_read_b64_tr_b16 a[84:85], v7 offset:57344\n"
        "ds_read_b64_tr_b16 a[86:87], v7 offset:57856\n"
        "v_fma_f32 v225, v246, v227, v157\n"
        "v_fma_f32 v224, -v247, v227, v224\n"
        "v_fma_f32 v225, v247, v226, v225\n"
        "v_fma_f32 v226, v246, v224, v142\n"
        "v_mfma_f32_32x32x16_bf16 v[160:175], a[240:243], v[76:79], v[160:175]\n"
        "ds_read_b64_tr_b16 a[240:241], v6 offset:51200\n"
        "ds_read_b64_tr_b16 a[242:243], v6 offset:51712\n"
        "buffer_load_dwordx4 v10, s[52:55], s13 offen offset:2048 lds\n"
        "v_fma_f32 v227, v246, v225, v158\n"
        "v_fma_f32 v226, -v247, v225, v226\n"
        "v_fma_f32 v227, v247, v224, v227\n"
        "v_mfma_f32_32x32x16_bf16 v[176:191], a[244:247], v[76:79], v[176:191]\n"
        "ds_read_b64_tr_b16 a[244:245], v7 offset:51200\n"
        "ds_read_b64_tr_b16 a[246:247], v7 offset:51712\n"
        "v_fma_f32 v224, v246, v226, v143\n"
        "v_fma_f32 v225, v246, v227, v159\n"
        "v_fma_f32 v224, -v247, v227, v224\n"
        "v_fma_f32 v225, v247, v226, v225\n"
        "v_mfma_f32_32x32x16_bf16 v[192:207], a[248:251], v[76:79], v[192:207]\n"
        "ds_read_b64_tr_b16 a[248:249], v6 offset:59392\n"
        "ds_read_b64_tr_b16 a[250:251], v6 offset:59904\n"
        "buffer_load_dwordx4 v11, s[52:55], s13 offen offset:3072 lds\n"
        "v_accvgpr_read_b32 v228, a99\n"
        "ds_write_b64 v228, v[224:225]\n"
        "v_mfma_f32_32x32x16_bf16 v[208:223], a[252:255], v[76:79], v[208:223]\n"
        "ds_read_b64_tr_b16 a[252:253], v7 offset:59392\n"
        "ds_read_b64_tr_b16 a[254:255], v7 offset:59904\n"
        "s_waitcnt lgkmcnt(9)\n"
        "v_mfma_f32_32x32x16_bf16 v[160:175], a[72:75], v[80:83], v[160:175]\n"
        "ds_read_b64_tr_b16 a[72:73], v6 offset:53248\n"
        "ds_read_b64_tr_b16 a[74:75], v6 offset:53760\n"
        "v_mfma_f32_32x32x16_bf16 v[176:191], a[76:79], v[80:83], v[176:191]\n"
        "ds_read_b64_tr_b16 a[76:77], v7 offset:53248\n"
        "ds_read_b64_tr_b16 a[78:79], v7 offset:53760\n"
        "v_mfma_f32_32x32x16_bf16 v[192:207], a[80:83], v[80:83], v[192:207]\n"
        "ds_read_b64_tr_b16 a[80:81], v6 offset:61440\n"
        "ds_read_b64_tr_b16 a[82:83], v6 offset:61952\n"
        "v_mfma_f32_32x32x16_bf16 v[208:223], a[84:87], v[80:83], v[208:223]\n"
        "ds_read_b64_tr_b16 a[84:85], v7 offset:61440\n"
        "ds_read_b64_tr_b16 a[86:87], v7 offset:61952\n"
        "s_waitcnt lgkmcnt(8)\n"
        "v_mfma_f32_32x32x16_bf16 v[160:175], a[240:243], v[84:87], v[160:175]\n"
        "ds_read_b64_tr_b16 a[240:241], v6 offset:55296\n"
        "ds_read_b64_tr_b16 a[242:243], v6 offset:55808\n"
        "v_mfma_f32_32x32x16_bf16 v[176:191], a[244:247], v[84:87], v[176:191]\n"
        "ds_read_b64_tr_b16 a[244:245], v7 offset:55296\n"
        "ds_read_b64_tr_b16 a[246:247], v7 offset:55808\n"
        "v_mfma_f32_32x32x16_bf16 v[192:207], a[248:251], v[84:87], v[192:207]\n"
        "ds_read_b64_tr_b16 a[248:249], v6 offset:63488\n"
        "ds_read_b64_tr_b16 a[250:251], v6 offset:64000\n"
        "v_mfma_f32_32x32x16_bf16 v[208:223], a[252:255], v[84:87], v[208:223]\n"
        "ds_read_b64_tr_b16 a[252:253], v7 offset:63488\n"
        "ds_read_b64_tr_b16 a[254:255], v7 offset:64000\n"
        "s_waitcnt lgkmcnt(8)\n"
        "v_mfma_f32_32x32x16_bf16 v[160:175], a[72:75], v[88:91], v[160:175]\n"
        "v_mfma_f32_32x32x16_bf16 v[176:191], a[76:79], v[88:91], v[176:191]\n"
        "s_waitcnt vmcnt(5) lgkmcnt(0)\n"
        "s_barrier\n"
        "v_accvgpr_read_b32 v254, a107\n"
        "buffer_store_dwordx2 v[14:15], v254, s[56:59], 0 offen sc1\n"
        "v_add_u32 v6, 65536, v6\n"
        "ds_read_b64_tr_b16 a[72:73], v6\n"
        "ds_read_b64_tr_b16 a[74:75], v6 offset:512\n"
        "v_add_u32 v7, 65536, v7\n"
        "ds_read_b64_tr_b16 a[76:77], v7\n"
        "ds_read_b64_tr_b16 a[78:79], v7 offset:512\n"
        "s_add_u32 m0, s72, 49152\n"
        "s_nop 0\n"
        "buffer_load_dwordx4 v8, s[52:55], s14 offen lds\n"
        "v_mfma_f32_32x32x16_bf16 v[192:207], a[80:83], v[88:91], v[192:207]\n"
        "ds_read_b64_tr_b16 a[80:81], v6 offset:8192\n"
        "ds_read_b64_tr_b16 a[82:83], v6 offset:8704\n"
        "v_accvgpr_read_b32 v228, a98\n"
        "ds_read_b64 v[16:17], v228\n"
        "ds_read_b64 v[18:19], v228 offset:256\n"
        "ds_read_b64 v[20:21], v228 offset:512\n"
        "v_mfma_f32_32x32x16_bf16 v[208:223], a[84:87], v[88:91], v[208:223]\n"
        "ds_read_b64_tr_b16 a[84:85], v7 offset:8192\n"
        "ds_read_b64_tr_b16 a[86:87], v7 offset:8704\n"
        "buffer_load_dwordx4 v9, s[52:55], s14 offen offset:1024 lds\n"
        "ds_read_b64 v[22:23], v228 offset:768\n"
        "ds_read_b64 v[24:25], v228 offset:1024\n"
        "ds_read_b64 v[26:27], v228 offset:1280\n"
        "v_mfma_f32_32x32x16_bf16 v[160:175], a[240:243], v[92:95], v[160:175]\n"
        "ds_read_b64_tr_b16 a[240:241], v6 offset:2048\n"
        "ds_read_b64_tr_b16 a[242:243], v6 offset:2560\n"
        "buffer_load_dwordx4 v10, s[52:55], s14 offen offset:2048 lds\n"
        "ds_read_b64 v[28:29], v228 offset:1536\n"
        "ds_read_b64 v[30:31], v228 offset:1792\n"
        "v_accvgpr_read_b32 v246, a119\n"
        "v_mfma_f32_32x32x16_bf16 v[176:191], a[244:247], v[92:95], v[176:191]\n"
        "ds_read_b64_tr_b16 a[244:245], v7 offset:2048\n"
        "ds_read_b64_tr_b16 a[246:247], v7 offset:2560\n"
        "v_accvgpr_read_b32 v247, a120\n"
        "v_accvgpr_read_b32 v229, a97\n"
        "v_mov_b32 v12, 0\n"
        "v_mov_b32 v13, 0\n"
        "v_mfma_f32_32x32x16_bf16 v[192:207], a[248:251], v[92:95], v[192:207]\n"
        "ds_read_b64_tr_b16 a[248:249], v6 offset:10240\n"
        "ds_read_b64_tr_b16 a[250:251], v6 offset:10752\n"
        "buffer_load_dwordx4 v11, s[52:55], s14 offen offset:3072 lds\n"
        "v_cmp_eq_u32 vcc, 1, v229\n"
        "s_waitcnt lgkmcnt(14)\n"
        "v_fma_f32 v224, v246, v16, v18\n"
        "v_mfma_f32_32x32x16_bf16 v[208:223], a[252:255], v[92:95], v[208:223]\n"
        "ds_read_b64_tr_b16 a[252:253], v7 offset:10240\n"
        "ds_read_b64_tr_b16 a[254:255], v7 offset:10752\n"
        "v_fma_f32 v225, v246, v17, v19\n"
        "v_cndmask_b32 v12, v12, v16, vcc\n"
        "v_cndmask_b32 v13, v13, v17, vcc\n"
        "v_fma_f32 v224, -v247, v17, v224\n"
        "s_waitcnt lgkmcnt(13)\n"
        "v_mfma_f32_32x32x16_bf16 v[160:175], a[72:75], v[96:99], v[160:175]\n"
        "ds_read_b64_tr_b16 a[72:73], v6 offset:4096\n"
        "ds_read_b64_tr_b16 a[74:75], v6 offset:4608\n"
        "v_fma_f32 v225, v247, v16, v225\n"
        "v_cmp_eq_u32 vcc, 2, v229\n"
        "v_fma_f32 v226, v246, v224, v20\n"
        "v_mfma_f32_32x32x16_bf16 v[176:191], a[76:79], v[96:99], v[176:191]\n"
        "ds_read_b64_tr_b16 a[76:77], v7 offset:4096\n"
        "ds_read_b64_tr_b16 a[78:79], v7 offset:4608\n"
        "v_fma_f32 v227, v246, v225, v21\n"
        "v_cndmask_b32 v12, v12, v224, vcc\n"
        "v_cndmask_b32 v13, v13, v225, vcc\n"
        "v_fma_f32 v226, -v247, v225, v226\n"
        "v_mfma_f32_32x32x16_bf16 v[192:207], a[80:83], v[96:99], v[192:207]\n"
        "ds_read_b64_tr_b16 a[80:81], v6 offset:12288\n"
        "ds_read_b64_tr_b16 a[82:83], v6 offset:12800\n"
        "v_fma_f32 v227, v247, v224, v227\n"
        "v_cmp_eq_u32 vcc, 3, v229\n"
        "s_waitcnt lgkmcnt(15)\n"
        "v_fma_f32 v224, v246, v226, v22\n"
        "v_mfma_f32_32x32x16_bf16 v[208:223], a[84:87], v[96:99], v[208:223]\n"
        "ds_read_b64_tr_b16 a[84:85], v7 offset:12288\n"
        "ds_read_b64_tr_b16 a[86:87], v7 offset:12800\n"
        "v_fma_f32 v225, v246, v227, v23\n"
        "v_cndmask_b32 v12, v12, v226, vcc\n"
        "v_cndmask_b32 v13, v13, v227, vcc\n"
        "v_fma_f32 v224, -v247, v227, v224\n"
        "s_waitcnt lgkmcnt(8)\n"
        "v_mfma_f32_32x32x16_bf16 v[160:175], a[240:243], v[100:103], v[160:175]\n"
        "ds_read_b64_tr_b16 a[240:241], v6 offset:6144\n"
        "ds_read_b64_tr_b16 a[242:243], v6 offset:6656\n"
        "v_fma_f32 v225, v247, v226, v225\n"
        "v_cmp_eq_u32 vcc, 4, v229\n"
        "v_fma_f32 v226, v246, v224, v24\n"
        "v_mfma_f32_32x32x16_bf16 v[176:191], a[244:247], v[100:103], v[176:191]\n"
        "ds_read_b64_tr_b16 a[244:245], v7 offset:6144\n"
        "ds_read_b64_tr_b16 a[246:247], v7 offset:6656\n"
        "v_fma_f32 v227, v246, v225, v25\n"
        "v_cndmask_b32 v12, v12, v224, vcc\n"
        "v_cndmask_b32 v13, v13, v225, vcc\n"
        "v_fma_f32 v226, -v247, v225, v226\n"
        "v_mfma_f32_32x32x16_bf16 v[192:207], a[248:251], v[100:103], v[192:207]\n"
        "ds_read_b64_tr_b16 a[248:249], v6 offset:14336\n"
        "ds_read_b64_tr_b16 a[250:251], v6 offset:14848\n"
        "v_fma_f32 v227, v247, v224, v227\n"
        "v_cmp_eq_u32 vcc, 5, v229\n"
        "v_fma_f32 v224, v246, v226, v26\n"
        "v_fma_f32 v225, v246, v227, v27\n"
        "v_mfma_f32_32x32x16_bf16 v[208:223], a[252:255], v[100:103], v[208:223]\n"
        "ds_read_b64_tr_b16 a[252:253], v7 offset:14336\n"
        "ds_read_b64_tr_b16 a[254:255], v7 offset:14848\n"
        "v_cndmask_b32 v12, v12, v226, vcc\n"
        "v_cndmask_b32 v13, v13, v227, vcc\n"
        "v_fma_f32 v224, -v247, v227, v224\n"
        "v_fma_f32 v225, v247, v226, v225\n"
        "s_waitcnt lgkmcnt(8)\n"
        "v_mfma_f32_32x32x16_bf16 v[160:175], a[72:75], v[104:107], v[160:175]\n"
        "v_cmp_eq_u32 vcc, 6, v229\n"
        "v_fma_f32 v226, v246, v224, v28\n"
        "v_fma_f32 v227, v246, v225, v29\n"
        "v_cndmask_b32 v12, v12, v224, vcc\n"
        "v_cndmask_b32 v13, v13, v225, vcc\n"
        "v_mfma_f32_32x32x16_bf16 v[176:191], a[76:79], v[104:107], v[176:191]\n"
        "s_waitcnt lgkmcnt(0)\n"
        "s_barrier\n"
        "ds_read_b64_tr_b16 a[72:73], v6 offset:16384\n"
        "ds_read_b64_tr_b16 a[74:75], v6 offset:16896\n"
        "ds_read_b64_tr_b16 a[76:77], v7 offset:16384\n"
        "ds_read_b64_tr_b16 a[78:79], v7 offset:16896\n"
        "s_add_u32 s8, s8, 57344\n"
        "s_add_u32 m0, s72, 65536\n"
        "s_nop 0\n"
        "buffer_load_dwordx4 v8, s[52:55], s8 offen lds\n"
        "v_mfma_f32_32x32x16_bf16 v[192:207], a[80:83], v[104:107], v[192:207]\n"
        "ds_read_b64_tr_b16 a[80:81], v6 offset:24576\n"
        "ds_read_b64_tr_b16 a[82:83], v6 offset:25088\n"
        "buffer_load_dwordx4 v9, s[52:55], s8 offen offset:1024 lds\n"
        "v_fma_f32 v226, -v247, v225, v226\n"
        "v_fma_f32 v227, v247, v224, v227\n"
        "v_cmp_eq_u32 vcc, 7, v229\n"
        "v_mfma_f32_32x32x16_bf16 v[208:223], a[84:87], v[104:107], v[208:223]\n"
        "ds_read_b64_tr_b16 a[84:85], v7 offset:24576\n"
        "ds_read_b64_tr_b16 a[86:87], v7 offset:25088\n"
        "v_fma_f32 v238, v246, v226, v30\n"
        "v_fma_f32 v240, v246, v227, v31\n"
        "v_cndmask_b32 v12, v12, v226, vcc\n"
        "v_cndmask_b32 v13, v13, v227, vcc\n"
        "v_mfma_f32_32x32x16_bf16 v[160:175], a[240:243], v[108:111], v[160:175]\n"
        "ds_read_b64_tr_b16 a[240:241], v6 offset:18432\n"
        "ds_read_b64_tr_b16 a[242:243], v6 offset:18944\n"
        "buffer_load_dwordx4 v10, s[52:55], s8 offen offset:2048 lds\n"
        "v_fma_f32 v238, -v247, v227, v238\n"
        "v_fma_f32 v240, v247, v226, v240\n"
        "v_mov_b32 v239, s96\n"
        "v_mfma_f32_32x32x16_bf16 v[176:191], a[244:247], v[108:111], v[176:191]\n"
        "ds_read_b64_tr_b16 a[244:245], v7 offset:18432\n"
        "ds_read_b64_tr_b16 a[246:247], v7 offset:18944\n"
        "v_mov_b32 v241, s96\n"
        "v_accvgpr_read_b32 v228, a106\n"
        "buffer_store_dwordx4 v[238:241], v228, s[56:59], 0 offen sc1\n"
        "v_mfma_f32_32x32x16_bf16 v[192:207], a[248:251], v[108:111], v[192:207]\n"
        "ds_read_b64_tr_b16 a[248:249], v6 offset:26624\n"
        "ds_read_b64_tr_b16 a[250:251], v6 offset:27136\n"
        "v_mfma_f32_32x32x16_bf16 v[208:223], a[252:255], v[108:111], v[208:223]\n"
        "ds_read_b64_tr_b16 a[252:253], v7 offset:26624\n"
        "ds_read_b64_tr_b16 a[254:255], v7 offset:27136\n"
        "s_waitcnt lgkmcnt(8)\n"
        "v_mfma_f32_32x32x16_bf16 v[160:175], a[72:75], v[112:115], v[160:175]\n"
        "ds_read_b64_tr_b16 a[72:73], v6 offset:20480\n"
        "ds_read_b64_tr_b16 a[74:75], v6 offset:20992\n"
        "buffer_load_dwordx4 v11, s[52:55], s8 offen offset:3072 lds\n"
        "v_mfma_f32_32x32x16_bf16 v[176:191], a[76:79], v[112:115], v[176:191]\n"
        "ds_read_b64_tr_b16 a[76:77], v7 offset:20480\n"
        "ds_read_b64_tr_b16 a[78:79], v7 offset:20992\n"
        "v_mfma_f32_32x32x16_bf16 v[192:207], a[80:83], v[112:115], v[192:207]\n"
        "ds_read_b64_tr_b16 a[80:81], v6 offset:28672\n"
        "ds_read_b64_tr_b16 a[82:83], v6 offset:29184\n"
        "v_mfma_f32_32x32x16_bf16 v[208:223], a[84:87], v[112:115], v[208:223]\n"
        "ds_read_b64_tr_b16 a[84:85], v7 offset:28672\n"
        "ds_read_b64_tr_b16 a[86:87], v7 offset:29184\n"
        "s_waitcnt lgkmcnt(8)\n"
        "v_mfma_f32_32x32x16_bf16 v[160:175], a[240:243], v[116:119], v[160:175]\n"
        "ds_read_b64_tr_b16 a[240:241], v6 offset:22528\n"
        "ds_read_b64_tr_b16 a[242:243], v6 offset:23040\n"
        "v_mfma_f32_32x32x16_bf16 v[176:191], a[244:247], v[116:119], v[176:191]\n"
        "ds_read_b64_tr_b16 a[244:245], v7 offset:22528\n"
        "ds_read_b64_tr_b16 a[246:247], v7 offset:23040\n"
        "v_mfma_f32_32x32x16_bf16 v[192:207], a[248:251], v[116:119], v[192:207]\n"
        "ds_read_b64_tr_b16 a[248:249], v6 offset:30720\n"
        "ds_read_b64_tr_b16 a[250:251], v6 offset:31232\n"
        "v_mfma_f32_32x32x16_bf16 v[208:223], a[252:255], v[116:119], v[208:223]\n"
        "ds_read_b64_tr_b16 a[252:253], v7 offset:30720\n"
        "ds_read_b64_tr_b16 a[254:255], v7 offset:31232\n"
        "s_waitcnt lgkmcnt(8)\n"
        "v_mfma_f32_32x32x16_bf16 v[160:175], a[72:75], v[120:123], v[160:175]\n"
        "v_mfma_f32_32x32x16_bf16 v[176:191], a[76:79], v[120:123], v[176:191]\n"
        "s_waitcnt lgkmcnt(0)\n"
        "s_barrier\n"
        "v_accvgpr_read_b32 v6, a91\n"
        "v_accvgpr_read_b32 v7, a92\n"
        "v_add_u32 v6, 65536, v6\n"
        "ds_read_b64_tr_b16 a[48:49], v6 offset:32768\n"
        "ds_read_b64_tr_b16 a[50:51], v6 offset:33792\n"
        "s_add_u32 s9, s9, 57344\n"
        "s_add_u32 m0, s72, 81920\n"
        "s_nop 0\n"
        "buffer_load_dwordx4 v8, s[52:55], s9 offen lds\n"
        "v_mfma_f32_32x32x16_bf16 v[192:207], a[80:83], v[120:123], v[192:207]\n"
        "v_add_u32 v7, 65536, v7\n"
        "ds_read_b64_tr_b16 a[52:53], v7 offset:32768\n"
        "ds_read_b64_tr_b16 a[54:55], v7 offset:33792\n"
        "buffer_load_dwordx4 v9, s[52:55], s9 offen offset:1024 lds\n"
        "v_mfma_f32_32x32x16_bf16 v[208:223], a[84:87], v[120:123], v[208:223]\n"
        "v_mfma_f32_32x32x16_bf16 v[160:175], a[240:243], v[124:127], v[160:175]\n"
        "buffer_load_dwordx4 v10, s[52:55], s9 offen offset:2048 lds\n"
        "v_mfma_f32_32x32x16_bf16 v[192:207], a[248:251], v[124:127], v[192:207]\n"
        "ds_read_b64_tr_b16 a[56:57], v6 offset:34816\n"
        "ds_read_b64_tr_b16 a[58:59], v6 offset:35840\n"
        "v_mfma_f32_32x32x16_bf16 v[176:191], a[244:247], v[124:127], v[176:191]\n"
        "ds_read_b64_tr_b16 a[60:61], v7 offset:34816\n"
        "ds_read_b64_tr_b16 a[62:63], v7 offset:35840\n"
        "buffer_load_dwordx4 v11, s[52:55], s9 offen offset:3072 lds\n"
        "v_mfma_f32_32x32x16_bf16 v[208:223], a[252:255], v[124:127], v[208:223]\n"
        "v_accvgpr_read_b32 v44, a102\n"
        "buffer_load_dwordx4 v[36:39], v44, s[56:59], 0 offen sc1\n"
        "v_accvgpr_read_b32 v45, a104\n"
        "buffer_load_dwordx4 v[40:43], v45, s[56:59], 0 offen sc1\n"
        "v_accvgpr_read_b32 v34, a108\n"
        "buffer_load_dwordx2 v[32:33], v34, s[56:59], 0 offen sc1\n"
        "v_accvgpr_read_b32 v229, a95\n"
        "s_mov_b32 s88, 4096\n"
        "v_max_f32 v230, 0, v160\n"
        "v_max_f32 v232, 0, v161\n"
        "v_mul_f32 v230, v230, v192\n"
        "v_mul_f32 v232, v232, v193\n"
        "v_cvt_pk_bf16_f32 v238, v230, v232\n"
        "v_max_f32 v234, 0, v162\n"
        "v_max_f32 v236, 0, v163\n"
        "v_mul_f32 v234, v234, v194\n"
        "v_mul_f32 v236, v236, v195\n"
        "v_cvt_pk_bf16_f32 v239, v234, v236\n"
        "v_max_f32 v230, 0, v164\n"
        "v_max_f32 v232, 0, v165\n"
        "v_mul_f32 v230, v230, v196\n"
        "v_mul_f32 v232, v232, v197\n"
        "v_cvt_pk_bf16_f32 v240, v230, v232\n"
        "v_max_f32 v234, 0, v166\n"
        "v_max_f32 v236, 0, v167\n"
        "v_mul_f32 v234, v234, v198\n"
        "v_mul_f32 v236, v236, v199\n"
        "v_cvt_pk_bf16_f32 v241, v234, v236\n"
        "buffer_store_dwordx4 v[238:241], v229, s[56:59], s88 offen sc1\n"
        "v_max_f32 v230, 0, v168\n"
        "v_max_f32 v232, 0, v169\n"
        "v_mul_f32 v230, v230, v200\n"
        "v_mul_f32 v232, v232, v201\n"
        "v_cvt_pk_bf16_f32 v242, v230, v232\n"
        "v_max_f32 v234, 0, v170\n"
        "v_max_f32 v236, 0, v171\n"
        "v_mul_f32 v234, v234, v202\n"
        "v_mul_f32 v236, v236, v203\n"
        "v_cvt_pk_bf16_f32 v243, v234, v236\n"
        "v_max_f32 v230, 0, v172\n"
        "v_max_f32 v232, 0, v173\n"
        "v_mul_f32 v230, v230, v204\n"
        "v_mul_f32 v232, v232, v205\n"
        "v_cvt_pk_bf16_f32 v244, v230, v232\n"
        "v_max_f32 v234, 0, v174\n"
        "v_max_f32 v236, 0, v175\n"
        "v_mul_f32 v234, v234, v206\n"
        "v_mul_f32 v236, v236, v207\n"
        "v_cvt_pk_bf16_f32 v245, v234, v236\n"
        "buffer_store_dwordx4 v[242:245], v229, s[56:59], s88 offen offset:1024 sc1\n"
        "v_max_f32 v230, 0, v176\n"
        "v_max_f32 v232, 0, v177\n"
        "v_mul_f32 v230, v230, v208\n"
        "v_mul_f32 v232, v232, v209\n"
        "v_cvt_pk_bf16_f32 v238, v230, v232\n"
        "v_max_f32 v234, 0, v178\n"
        "v_max_f32 v236, 0, v179\n"
        "v_mul_f32 v234, v234, v210\n"
        "v_mul_f32 v236, v236, v211\n"
        "v_cvt_pk_bf16_f32 v239, v234, v236\n"
        "v_max_f32 v230, 0, v180\n"
        "v_max_f32 v232, 0, v181\n"
        "v_mul_f32 v230, v230, v212\n"
        "v_mul_f32 v232, v232, v213\n"
        "v_cvt_pk_bf16_f32 v240, v230, v232\n"
        "v_max_f32 v234, 0, v182\n"
        "v_max_f32 v236, 0, v183\n"
        "v_mul_f32 v234, v234, v214\n"
        "v_mul_f32 v236, v236, v215\n"
        "v_cvt_pk_bf16_f32 v241, v234, v236\n"
        "buffer_store_dwordx4 v[238:241], v229, s[56:59], s88 offen offset:2048 sc1\n"
        "v_max_f32 v230, 0, v184\n"
        "v_max_f32 v232, 0, v185\n"
        "v_mul_f32 v230, v230, v216\n"
        "v_mul_f32 v232, v232, v217\n"
        "v_cvt_pk_bf16_f32 v242, v230, v232\n"
        "v_max_f32 v234, 0, v186\n"
        "v_max_f32 v236, 0, v187\n"
        "v_mul_f32 v234, v234, v218\n"
        "v_mul_f32 v236, v236, v219\n"
        "v_cvt_pk_bf16_f32 v243, v234, v236\n"
        "v_max_f32 v230, 0, v188\n"
        "v_max_f32 v232, 0, v189\n"
        "v_mul_f32 v230, v230, v220\n"
        "v_mul_f32 v232, v232, v221\n"
        "v_cvt_pk_bf16_f32 v244, v230, v232\n"
        "v_max_f32 v234, 0, v190\n"
        "v_max_f32 v236, 0, v191\n"
        "v_mul_f32 v234, v234, v222\n"
        "v_mul_f32 v236, v236, v223\n"
        "v_cvt_pk_bf16_f32 v245, v234, v236\n"
        "buffer_store_dwordx4 v[242:245], v229, s[56:59], s88 offen offset:3072 sc1\n"
        "v_accvgpr_read_b32 v46, a103\n"
        "v_accvgpr_read_b32 v47, a105\n"
        "s_waitcnt vmcnt(5)\n"
        "s_mov_b32 s99, 400000\n"
        ".Lp0_%=:\n"
        "v_xor_b32 v224, s96, v37\n"
        "v_xor_b32 v225, s96, v39\n"
        "v_or_b32 v224, v224, v225\n"
        "v_and_b32 v224, v224, v46\n"
        "v_xor_b32 v226, s96, v41\n"
        "v_xor_b32 v227, s96, v43\n"
        "v_or_b32 v226, v226, v227\n"
        "v_and_b32 v226, v226, v47\n"
        "v_or_b32 v224, v224, v226\n"
        "v_cmp_eq_u32 vcc, 0, v224\n"
        "s_cmp_eq_u64 vcc, -1\n"
        "s_cbranch_scc1 .Lq0_%=\n"
        "s_sub_u32 s99, s99, 1\n"
        "s_cmp_eq_u32 s99, 0\n"
        "s_cbranch_scc1 .Lq0_%=\n"
        "s_sleep 1\n"
        "buffer_load_dwordx4 v[36:39], v44, s[56:59], 0 offen sc1\n"
        "buffer_load_dwordx4 v[40:43], v45, s[56:59], 0 offen sc1\n"
        "s_waitcnt vmcnt(0)\n"
        "s_nop 1\n"
        "s_branch .Lp0_%=\n"
        ".Lq0_%=:\n"
        "v_accvgpr_read_b32 v228, a101\n"
        "ds_write2_b32 v228, v36, v38 offset0:0 offset1:1\n"
        "ds_write2_b32 v228, v40, v42 offset0:128 offset1:129\n"
        "s_waitcnt lgkmcnt(0)\n"
        "s_barrier\n"
        "s_waitcnt vmcnt(4)\n"
        "s_mov_b32 s99, 400000\n"
        ".Lp1_%=:\n"
        "v_cmp_eq_u64 vcc, s[96:97], v[32:33]\n"
        "s_cmp_eq_u64 vcc, -1\n"
        "s_cbranch_scc1 .Lq1_%=\n"
        "s_sub_u32 s99, s99, 1\n"
        "s_cmp_eq_u32 s99, 0\n"
        "s_cbranch_scc1 .Lq1_%=\n"
        "s_sleep 1\n"
        "buffer_load_dwordx2 v[32:33], v34, s[56:59], 0 offen sc1\n"
        "s_waitcnt vmcnt(0)\n"
        "s_nop 1\n"
        "s_branch .Lp1_%=\n"
        ".Lq1_%=:\n"
        "v_accvgpr_read_b32 v228, a100\n"
        "ds_read_b64 v[200:201], v228\n"
        "ds_read_b64 v[202:203], v228 offset:256\n"
        "ds_read_b64 v[204:205], v228 offset:512\n"
        "ds_read_b64 v[206:207], v228 offset:768\n"
        "ds_read_b64 v[208:209], v228 offset:1024\n"
        "ds_read_b64 v[210:211], v228 offset:1280\n"
        "ds_read_b64 v[212:213], v228 offset:1536\n"
        "ds_read_b64 v[214:215], v228 offset:1792\n"
        "v_accvgpr_read_b32 v246, a123\n"
        "v_accvgpr_read_b32 v247, a124\n"
        "v_accvgpr_read_b32 v230, a125\n"
        "buffer_load_dwordx4 a[128:131], v1, s[56:59], s15 offen sc1\n"
        "v_accvgpr_read_b32 v231, a126\n"
        "v_accvgpr_read_b32 v232, a121\n"
        "v_accvgpr_read_b32 v233, a122\n"
        "s_waitcnt lgkmcnt(6)\n"
        "v_fmac_f32 v202, v246, v200\n"
        "v_fmac_f32 v203, v246, v201\n"
        "v_fma_f32 v202, -v247, v201, v202\n"
        "v_fmac_f32 v203, v247, v200\n"
        "s_waitcnt lgkmcnt(5)\n"
        "v_fmac_f32 v204, v246, v202\n"
        "v_fmac_f32 v205, v246, v203\n"
        "v_fma_f32 v204, -v247, v203, v204\n"
        "buffer_load_dwordx4 a[132:135], v1, s[56:59], s15 offen offset:1024 sc1\n"
        "v_fmac_f32 v205, v247, v202\n"
        "s_waitcnt lgkmcnt(4)\n"
        "v_fmac_f32 v206, v246, v204\n"
        "v_fmac_f32 v207, v246, v205\n"
        "v_fma_f32 v206, -v247, v205, v206\n"
        "v_fmac_f32 v207, v247, v204\n"
        "s_waitcnt lgkmcnt(3)\n"
        "v_fmac_f32 v208, v246, v206\n"
        "v_fmac_f32 v209, v246, v207\n"
        "v_fma_f32 v208, -v247, v207, v208\n"
        "v_fmac_f32 v209, v247, v206\n"
        "s_waitcnt lgkmcnt(2)\n"
        "v_fmac_f32 v210, v246, v208\n"
        "buffer_load_dwordx4 a[136:139], v1, s[56:59], s15 offen offset:2048 sc1\n"
        "v_fmac_f32 v211, v246, v209\n"
        "v_fma_f32 v210, -v247, v209, v210\n"
        "v_fmac_f32 v211, v247, v208\n"
        "s_waitcnt lgkmcnt(1)\n"
        "v_fmac_f32 v212, v246, v210\n"
        "v_fmac_f32 v213, v246, v211\n"
        "v_fma_f32 v212, -v247, v211, v212\n"
        "v_fmac_f32 v213, v247, v210\n"
        "s_waitcnt lgkmcnt(0)\n"
        "v_fmac_f32 v214, v246, v212\n"
        "s_waitcnt vmcnt(3)\n"
        "s_barrier\n"
        "v_accvgpr_read_b32 v254, a107\n"
        "buffer_store_dwordx2 v[14:15], v254, s[56:59], 0 offen offset:1792 sc1\n"
        "v_fmac_f32 v215, v246, v213\n"
        "v_fma_f32 v214, -v247, v213, v214\n"
        "v_fmac_f32 v215, v247, v212\n"
        "v_mov_b32 v224, v214\n"
        "v_mov_b32 v225, v215\n"
        "v_accvgpr_read_b32 v246, a115\n"
        "v_accvgpr_read_b32 v247, a116\n"
        "v_permlane32_swap_b32 v214, v224\n"
        "v_permlane32_swap_b32 v215, v225\n"
        "v_accvgpr_read_b32 v234, a117\n"
        "v_accvgpr_read_b32 v235, a118\n"
        "v_fmac_f32 v224, v230, v214\n"
        "buffer_load_dwordx4 a[140:143], v1, s[56:59], s15 offen offset:3072 sc1\n"
        "v_fmac_f32 v225, v230, v215\n"
        "v_fma_f32 v224, -v231, v215, v224\n"
        "v_fmac_f32 v225, v231, v214\n"
        "v_fmac_f32 v12, v232, v224\n"
        "v_fmac_f32 v13, v232, v225\n"
        "v_fma_f32 v12, -v233, v225, v12\n"
        "v_fmac_f32 v13, v233, v224\n"
        "v_fmac_f32 v128, v246, v12\n"
        "v_fmac_f32 v144, v246, v13\n"
        "v_fma_f32 v128, -v247, v13, v128\n"
        "v_fmac_f32 v144, v247, v12\n"
        "v_fmac_f32 v129, v246, v128\n"
        "buffer_load_dwordx4 a[144:147], v1, s[56:59], s16 offen sc1\n"
        "v_fmac_f32 v145, v246, v144\n"
        "v_fma_f32 v129, -v247, v144, v129\n"
        "v_fmac_f32 v145, v247, v128\n"
        "v_mul_f32 v236, v128, v235\n"
        "v_mul_f32 v128, v128, v234\n"
        "v_fma_f32 v128, -v144, v235, v128\n"
        "v_fma_f32 v144, -v144, v234, -v236\n"
        "v_fmac_f32 v130, v246, v129\n"
        "v_fmac_f32 v146, v246, v145\n"
        "v_fma_f32 v130, -v247, v145, v130\n"
        "v_fmac_f32 v146, v247, v129\n"
        "v_mul_f32 v237, v129, v235\n"
        "buffer_load_dwordx4 a[148:151], v1, s[56:59], s16 offen offset:1024 sc1\n"
        "v_mul_f32 v129, v129, v234\n"
        "v_fma_f32 v129, -v145, v235, v129\n"
        "v_fma_f32 v145, -v145, v234, -v237\n"
        "v_fmac_f32 v131, v246, v130\n"
        "v_fmac_f32 v147, v246, v146\n"
        "v_fma_f32 v131, -v247, v146, v131\n"
        "v_fmac_f32 v147, v247, v130\n"
        "v_mul_f32 v236, v130, v235\n"
        "v_mul_f32 v130, v130, v234\n"
        "v_fma_f32 v130, -v146, v235, v130\n"
        "v_fma_f32 v146, -v146, v234, -v236\n"
        "v_fmac_f32 v132, v246, v131\n"
        "buffer_load_dwordx4 a[152:155], v1, s[56:59], s16 offen offset:2048 sc1\n"
        "v_fmac_f32 v148, v246, v147\n"
        "v_fma_f32 v132, -v247, v147, v132\n"
        "v_fmac_f32 v148, v247, v131\n"
        "v_mul_f32 v237, v131, v235\n"
        "v_mul_f32 v131, v131, v234\n"
        "v_fma_f32 v131, -v147, v235, v131\n"
        "v_fma_f32 v147, -v147, v234, -v237\n"
        "v_fmac_f32 v133, v246, v132\n"
        "v_fmac_f32 v149, v246, v148\n"
        "v_fma_f32 v133, -v247, v148, v133\n"
        "v_fmac_f32 v149, v247, v132\n"
        "v_mul_f32 v236, v132, v235\n"
        "buffer_load_dwordx4 a[156:159], v1, s[56:59], s16 offen offset:3072 sc1\n"
        "v_mul_f32 v132, v132, v234\n"
        "v_fma_f32 v132, -v148, v235, v132\n"
        "v_fma_f32 v148, -v148, v234, -v236\n"
        "v_fmac_f32 v134, v246, v133\n"
        "v_fmac_f32 v150, v246, v149\n"
        "v_fma_f32 v134, -v247, v149, v134\n"
        "v_fmac_f32 v150, v247, v133\n"
        "v_mul_f32 v237, v133, v235\n"
        "v_mul_f32 v133, v133, v234\n"
        "v_fma_f32 v133, -v149, v235, v133\n"
        "v_fma_f32 v149, -v149, v234, -v237\n"
        "v_fmac_f32 v135, v246, v134\n"
        "buffer_load_dwordx4 a[160:163], v1, s[56:59], s17 offen sc1\n"
        "v_fmac_f32 v151, v246, v150\n"
        "v_fma_f32 v135, -v247, v150, v135\n"
        "v_fmac_f32 v151, v247, v134\n"
        "v_mul_f32 v236, v134, v235\n"
        "v_mul_f32 v134, v134, v234\n"
        "v_fma_f32 v134, -v150, v235, v134\n"
        "v_fma_f32 v150, -v150, v234, -v236\n"
        "v_fmac_f32 v136, v246, v135\n"
        "v_fmac_f32 v152, v246, v151\n"
        "v_fma_f32 v136, -v247, v151, v136\n"
        "v_fmac_f32 v152, v247, v135\n"
        "v_mul_f32 v237, v135, v235\n"
        "buffer_load_dwordx4 a[164:167], v1, s[56:59], s17 offen offset:1024 sc1\n"
        "v_mul_f32 v135, v135, v234\n"
        "v_fma_f32 v135, -v151, v235, v135\n"
        "v_fma_f32 v151, -v151, v234, -v237\n"
        "v_fmac_f32 v137, v246, v136\n"
        "v_fmac_f32 v153, v246, v152\n"
        "v_fma_f32 v137, -v247, v152, v137\n"
        "v_fmac_f32 v153, v247, v136\n"
        "v_mul_f32 v236, v136, v235\n"
        "v_mul_f32 v136, v136, v234\n"
        "v_fma_f32 v136, -v152, v235, v136\n"
        "v_fma_f32 v152, -v152, v234, -v236\n"
        "v_fmac_f32 v138, v246, v137\n"
        "buffer_load_dwordx4 a[168:171], v1, s[56:59], s17 offen offset:2048 sc1\n"
        "v_fmac_f32 v154, v246, v153\n"
        "v_fma_f32 v138, -v247, v153, v138\n"
        "v_fmac_f32 v154, v247, v137\n"
        "v_mul_f32 v237, v137, v235\n"
        "v_mul_f32 v137, v137, v234\n"
        "v_fma_f32 v137, -v153, v235, v137\n"
        "v_fma_f32 v153, -v153, v234, -v237\n"
        "v_fmac_f32 v139, v246, v138\n"
        "v_fmac_f32 v155, v246, v154\n"
        "v_fma_f32 v139, -v247, v154, v139\n"
        "v_fmac_f32 v155, v247, v138\n"
        "v_mul_f32 v236, v138, v235\n"
        "buffer_load_dwordx4 a[172:175], v1, s[56:59], s17 offen offset:3072 sc1\n"
        "v_mul_f32 v138, v138, v234\n"
        "v_fma_f32 v138, -v154, v235, v138\n"
        "v_fma_f32 v154, -v154, v234, -v236\n"
        "v_fmac_f32 v140, v246, v139\n"
        "v_fmac_f32 v156, v246, v155\n"
        "v_fma_f32 v140, -v247, v155, v140\n"
        "v_fmac_f32 v156, v247, v139\n"
        "v_mul_f32 v237, v139, v235\n"
        "v_mul_f32 v139, v139, v234\n"
        "v_fma_f32 v139, -v155, v235, v139\n"
        "v_fma_f32 v155, -v155, v234, -v237\n"
        "v_fmac_f32 v141, v246, v140\n"
        "buffer_load_dwordx4 a[176:179], v1, s[56:59], s18 offen sc1\n"
        "v_fmac_f32 v157, v246, v156\n"
        "v_fma_f32 v141, -v247, v156, v141\n"
        "v_fmac_f32 v157, v247, v140\n"
        "v_mul_f32 v236, v140, v235\n"
        "v_mul_f32 v140, v140, v234\n"
        "v_fma_f32 v140, -v156, v235, v140\n"
        "v_fma_f32 v156, -v156, v234, -v236\n"
        "v_fmac_f32 v142, v246, v141\n"
        "v_fmac_f32 v158, v246, v157\n"
        "v_fma_f32 v142, -v247, v157, v142\n"
        "v_fmac_f32 v158, v247, v141\n"
        "v_mul_f32 v237, v141, v235\n"
        "buffer_load_dwordx4 a[180:183], v1, s[56:59], s18 offen offset:1024 sc1\n"
        "v_mul_f32 v141, v141, v234\n"
        "v_fma_f32 v141, -v157, v235, v141\n"
        "v_fma_f32 v157, -v157, v234, -v237\n"
        "v_fmac_f32 v143, v246, v142\n"
        "v_fmac_f32 v159, v246, v158\n"
        "v_fma_f32 v143, -v247, v158, v143\n"
        "v_fmac_f32 v159, v247, v142\n"
        "v_mul_f32 v236, v142, v235\n"
        "v_mul_f32 v142, v142, v234\n"
        "v_fma_f32 v142, -v158, v235, v142\n"
        "v_fma_f32 v158, -v158, v234, -v236\n"
        "v_mul_f32 v237, v143, v235\n"
        "buffer_load_dwordx4 a[184:187], v1, s[56:59], s18 offen offset:2048 sc1\n"
        "v_mul_f32 v143, v143, v234\n"
        "v_fma_f32 v143, -v159, v235, v143\n"
        "v_fma_f32 v159, -v159, v234, -v237\n"
        "v_accvgpr_read_b32 v228, a93\n"
        "v_accvgpr_read_b32 v229, a94\n"
        "v_accvgpr_read_b32 v224, a96\n"
        "v_cvt_pk_bf16_f32 v230, v128, v129\n"
        "v_cvt_pk_bf16_f32 v231, v130, v131\n"
        "ds_write_b64 v228, v[230:231]\n"
        "v_cvt_pk_bf16_f32 v232, v132, v133\n"
        "v_cvt_pk_bf16_f32 v233, v134, v135\n"
        "ds_write_b64 v228, v[232:233] offset:16\n"
        "buffer_load_dwordx4 a[188:191], v1, s[56:59], s18 offen offset:3072 sc1\n"
        "v_cvt_pk_bf16_f32 v234, v136, v137\n"
        "v_cvt_pk_bf16_f32 v235, v138, v139\n"
        "ds_write_b64 v228, v[234:235] offset:32\n"
        "v_cvt_pk_bf16_f32 v236, v140, v141\n"
        "v_cvt_pk_bf16_f32 v237, v142, v143\n"
        "ds_write_b64 v228, v[236:237] offset:48\n"
        "ds_read_b64_tr_b16 v[238:239], v229\n"
        "ds_read_b64_tr_b16 v[240:241], v229 offset:288\n"
        "ds_read_b64_tr_b16 v[242:243], v229 offset:1152\n"
        "ds_read_b64_tr_b16 v[244:245], v229 offset:1440\n"
        "s_waitcnt lgkmcnt(2)\n"
        "buffer_store_dwordx4 v[238:241], v224, s[56:59], 0 offen sc1\n"
        "s_waitcnt lgkmcnt(0)\n"
        "buffer_store_dwordx4 v[242:245], v224, s[56:59], 0 offen offset:1024 sc1\n"
        "v_cvt_pk_bf16_f32 v230, v144, v145\n"
        "v_cvt_pk_bf16_f32 v231, v146, v147\n"
        "ds_write_b64 v228, v[230:231]\n"
        "v_cvt_pk_bf16_f32 v232, v148, v149\n"
        "v_cvt_pk_bf16_f32 v233, v150, v151\n"
        "ds_write_b64 v228, v[232:233] offset:16\n"
        "v_cvt_pk_bf16_f32 v234, v152, v153\n"
        "v_cvt_pk_bf16_f32 v235, v154, v155\n"
        "ds_write_b64 v228, v[234:235] offset:32\n"
        "v_cvt_pk_bf16_f32 v236, v156, v157\n"
        "v_cvt_pk_bf16_f32 v237, v158, v159\n"
        "ds_write_b64 v228, v[236:237] offset:48\n"
        "buffer_load_dwordx4 a[192:195], v1, s[56:59], s19 offen sc1\n"
        "ds_read_b64_tr_b16 v[128:129], v229\n"
        "ds_read_b64_tr_b16 v[130:131], v229 offset:288\n"
        "ds_read_b64_tr_b16 v[132:133], v229 offset:1152\n"
        "ds_read_b64_tr_b16 v[134:135], v229 offset:1440\n"
        "s_waitcnt lgkmcnt(2)\n"
        "buffer_store_dwordx4 v[128:131], v224, s[56:59], s88 offen sc1\n"
        "s_waitcnt lgkmcnt(0)\n"
        "buffer_store_dwordx4 v[132:135], v224, s[56:59], s88 offen offset:1024 sc1\n"
        "s_waitcnt vmcnt(21)\n"
        "v_mfma_f32_32x32x16_bf16 a[0:15], a[128:131], a[48:51], 0\n"
        "ds_read_b64_tr_b16 a[64:65], v6 offset:36864\n"
        "ds_read_b64_tr_b16 a[66:67], v6 offset:37888\n"
        "buffer_load_dwordx4 a[196:199], v1, s[56:59], s19 offen offset:1024 sc1\n"
        "buffer_load_dwordx4 a[200:203], v1, s[56:59], s19 offen offset:2048 sc1\n"
        "s_mul_i32 s95, s66, 12288\n"
        "v_add_u32 v192, s95, v2\n"
        "v_add_u32 v193, s95, v3\n"
        "v_add_u32 v194, s95, v4\n"
        "v_add_u32 v195, s95, v5\n"
        "v_mfma_f32_32x32x16_bf16 a[16:31], a[128:131], a[52:55], 0\n"
        "ds_read_b64_tr_b16 a[68:69], v7 offset:36864\n"
        "ds_read_b64_tr_b16 a[70:71], v7 offset:37888\n"
        "buffer_load_dwordx4 a[204:207], v1, s[56:59], s19 offen offset:3072 sc1\n"
        "buffer_load_dwordx4 a[208:211], v1, s[56:59], s20 offen sc1\n"
        "s_lshl_b32 s95, s66, 12\n"
        "s_add_u32 s95, s95, 4096\n"
        "v_add_u32 v196, s95, v2\n"
        "v_add_u32 v197, s95, v3\n"
        "v_add_u32 v198, s95, v4\n"
        "v_add_u32 v199, s95, v5\n"
        "s_waitcnt vmcnt(24)\n"
        "v_mfma_f32_32x32x16_bf16 a[0:15], a[132:135], a[56:59], a[0:15]\n"
        "ds_read_b64_tr_b16 a[72:73], v6 offset:38912\n"
        "ds_read_b64_tr_b16 a[74:75], v6 offset:39936\n"
        "buffer_load_dwordx4 a[212:215], v1, s[56:59], s20 offen offset:1024 sc1\n"
        "buffer_load_dwordx4 a[216:219], v1, s[56:59], s20 offen offset:2048 sc1\n"
        "s_mov_b32 s81, 3584\n"
        "s_mov_b32 s82, 7168\n"
        "s_mov_b32 s83, 10752\n"
        "s_mov_b32 s84, 14336\n"
        "s_mov_b32 s85, 17920\n"
        "s_mov_b32 s86, 21504\n"
        "s_mov_b32 s87, 25088\n"
        "v_mfma_f32_32x32x16_bf16 a[16:31], a[132:135], a[60:63], a[16:31]\n"
        "ds_read_b64_tr_b16 a[76:77], v7 offset:38912\n"
        "ds_read_b64_tr_b16 a[78:79], v7 offset:39936\n"
        "buffer_load_dwordx4 a[220:223], v1, s[56:59], s20 offen offset:3072 sc1\n"
        "buffer_load_dwordx4 a[224:227], v1, s[56:59], s21 offen sc1\n"
        "v_accvgpr_read_b32 v225, a111\n"
        "buffer_load_ushort v176, v225, s[36:39], 0 offen\n"
        "buffer_load_ushort v177, v225, s[36:39], 0 offen offset:896\n"
        "buffer_load_ushort v178, v225, s[36:39], 0 offen offset:1792\n"
        "buffer_load_ushort v179, v225, s[36:39], 0 offen offset:2688\n"
        "s_waitcnt vmcnt(31) lgkmcnt(4)\n"
        "v_mfma_f32_32x32x16_bf16 a[0:15], a[136:139], a[64:67], a[0:15]\n"
        "s_waitcnt lgkmcnt(0)\n"
        "s_barrier\n"
        "ds_read_b64_tr_b16 a[48:49], v6 offset:49152\n"
        "ds_read_b64_tr_b16 a[50:51], v6 offset:50176\n"
        "buffer_load_dwordx4 a[228:231], v1, s[56:59], s21 offen offset:1024 sc1\n"
        "buffer_load_ushort v180, v225, s[36:39], s81 offen\n"
        "buffer_load_ushort v181, v225, s[36:39], s81 offen offset:896\n"
        "buffer_load_ushort v182, v225, s[36:39], s81 offen offset:1792\n"
        "s_add_u32 s10, s10, 57344\n"
        "s_add_u32 m0, s72, 98304\n"
        "v_mfma_f32_32x32x16_bf16 a[16:31], a[136:139], a[68:71], a[16:31]\n"
        "ds_read_b64_tr_b16 a[52:53], v7 offset:49152\n"
        "ds_read_b64_tr_b16 a[54:55], v7 offset:50176\n"
        "buffer_load_dwordx4 a[232:235], v1, s[56:59], s21 offen offset:2048 sc1\n"
        "buffer_load_dwordx4 a[236:239], v1, s[56:59], s21 offen offset:3072 sc1\n"
        "buffer_load_ushort v183, v225, s[36:39], s81 offen offset:2688\n"
        "buffer_load_ushort v184, v225, s[36:39], s82 offen\n"
        "buffer_load_ushort v185, v225, s[36:39], s82 offen offset:896\n"
        "buffer_load_ushort v186, v225, s[36:39], s82 offen offset:1792\n"
        "buffer_load_ushort v187, v225, s[36:39], s82 offen offset:2688\n"
        "s_waitcnt vmcnt(40)\n"
        "v_mfma_f32_32x32x16_bf16 a[0:15], a[140:143], a[72:75], a[0:15]\n"
        "ds_read_b64_tr_b16 a[56:57], v6 offset:51200\n"
        "ds_read_b64_tr_b16 a[58:59], v6 offset:52224\n"
        "buffer_load_ushort v188, v225, s[36:39], s83 offen\n"
        "buffer_load_ushort v189, v225, s[36:39], s83 offen offset:896\n"
        "buffer_load_ushort v190, v225, s[36:39], s83 offen offset:1792\n"
        "buffer_load_ushort v191, v225, s[36:39], s83 offen offset:2688\n"
        "v_accvgpr_read_b32 v226, a112\n"
        "v_accvgpr_read_b32 v227, a114\n"
        "v_mfma_f32_32x32x16_bf16 a[16:31], a[140:143], a[76:79], a[16:31]\n"
        "ds_read_b64_tr_b16 a[60:61], v7 offset:51200\n"
        "ds_read_b64_tr_b16 a[62:63], v7 offset:52224\n"
        "ds_read_b128 v[230:233], v226\n"
        "ds_read_b128 v[234:237], v226 offset:16\n"
        "ds_read_b128 v[238:241], v226 offset:32\n"
        "ds_read_b128 v[242:245], v226 offset:48\n"
        "s_waitcnt vmcnt(18)\n"
        "v_lshlrev_b32 v176, 16, v176\n"
        "s_waitcnt vmcnt(17)\n"
        "v_lshlrev_b32 v177, 16, v177\n"
        "s_waitcnt lgkmcnt(8)\n"
        "v_mfma_f32_32x32x16_bf16 a[0:15], a[144:147], a[48:51], a[0:15]\n"
        "ds_read_b64_tr_b16 a[64:65], v6 offset:53248\n"
        "ds_read_b64_tr_b16 a[66:67], v6 offset:54272\n"
        "v_accvgpr_read_b32 v34, a108\n"
        "buffer_load_dwordx2 v[32:33], v34, s[56:59], 0 offen offset:1792 sc1\n"
        "buffer_load_dwordx4 v8, s[52:55], s10 offen lds\n"
        "s_waitcnt lgkmcnt(5)\n"
        "v_fma_f32 v246, v176, v230, v231\n"
        "v_fma_f32 v247, v177, v232, v233\n"
        "v_mfma_f32_32x32x16_bf16 a[16:31], a[144:147], a[52:55], a[16:31]\n"
        "ds_read_b64_tr_b16 a[68:69], v7 offset:53248\n"
        "ds_read_b64_tr_b16 a[70:71], v7 offset:54272\n"
        "buffer_load_dwordx4 v9, s[52:55], s10 offen offset:1024 lds\n"
        "ds_read_b128 v[230:233], v226 offset:64\n"
        "v_mul_f32 v160, v246, v227\n"
        "v_mul_f32 v161, v247, v227\n"
        "s_waitcnt vmcnt(19)\n"
        "v_lshlrev_b32 v178, 16, v178\n"
        "s_waitcnt vmcnt(18)\n"
        "v_lshlrev_b32 v179, 16, v179\n"
        "v_mfma_f32_32x32x16_bf16 a[0:15], a[148:151], a[56:59], a[0:15]\n"
        "ds_read_b64_tr_b16 a[72:73], v6 offset:55296\n"
        "ds_read_b64_tr_b16 a[74:75], v6 offset:56320\n"
        "buffer_load_dwordx4 v10, s[52:55], s10 offen offset:2048 lds\n"
        "s_waitcnt lgkmcnt(9)\n"
        "v_fma_f32 v246, v178, v234, v235\n"
        "v_fma_f32 v247, v179, v236, v237\n"
        "ds_read_b128 v[234:237], v226 offset:80\n"
        "v_mul_f32 v162, v246, v227\n"
        "v_mul_f32 v163, v247, v227\n"
        "v_mfma_f32_32x32x16_bf16 a[16:31], a[148:151], a[60:63], a[16:31]\n"
        "ds_read_b64_tr_b16 a[76:77], v7 offset:55296\n"
        "ds_read_b64_tr_b16 a[78:79], v7 offset:56320\n"
        "buffer_load_dwordx4 v11, s[52:55], s10 offen offset:3072 lds\n"
        "s_waitcnt vmcnt(18)\n"
        "v_lshlrev_b32 v180, 16, v180\n"
        "s_waitcnt vmcnt(17)\n"
        "v_lshlrev_b32 v181, 16, v181\n"
        "s_waitcnt lgkmcnt(11)\n"
        "v_fma_f32 v246, v180, v238, v239\n"
        "s_waitcnt lgkmcnt(6)\n"
        "v_mfma_f32_32x32x16_bf16 a[0:15], a[152:155], a[64:67], a[0:15]\n"
        "s_waitcnt lgkmcnt(0)\n"
        "s_barrier\n"
        "v_add_u32 v6, 65536, v6\n"
        "ds_read_b64_tr_b16 a[48:49], v6\n"
        "ds_read_b64_tr_b16 a[50:51], v6 offset:1024\n"
        "s_add_u32 s11, s11, 57344\n"
        "s_add_u32 m0, s72, 114688\n"
        "s_nop 0\n"
        "buffer_load_dwordx4 v8, s[52:55], s11 offen lds\n"
        "buffer_load_dwordx4 v9, s[52:55], s11 offen offset:1024 lds\n"
        "v_mfma_f32_32x32x16_bf16 a[16:31], a[152:155], a[68:71], a[16:31]\n"
        "v_add_u32 v7, 65536, v7\n"
        "ds_read_b64_tr_b16 a[52:53], v7\n"
        "ds_read_b64_tr_b16 a[54:55], v7 offset:1024\n"
        "buffer_load_dwordx4 v10, s[52:55], s11 offen offset:2048 lds\n"
        "v_fma_f32 v247, v181, v240, v241\n"
        "ds_read_b128 v[238:241], v226 offset:96\n"
        "v_mul_f32 v164, v246, v227\n"
        "v_mul_f32 v165, v247, v227\n"
        "s_waitcnt vmcnt(19)\n"
        "v_lshlrev_b32 v182, 16, v182\n"
        "v_mfma_f32_32x32x16_bf16 a[0:15], a[156:159], a[72:75], a[0:15]\n"
        "ds_read_b64_tr_b16 a[56:57], v6 offset:2048\n"
        "ds_read_b64_tr_b16 a[58:59], v6 offset:3072\n"
        "buffer_load_dwordx4 v11, s[52:55], s11 offen offset:3072 lds\n"
        "s_waitcnt vmcnt(17)\n"
        "v_lshlrev_b32 v183, 16, v183\n"
        "v_fma_f32 v246, v182, v242, v243\n"
        "v_fma_f32 v247, v183, v244, v245\n"
        "ds_read_b128 v[242:245], v226 offset:112\n"
        "v_mul_f32 v166, v246, v227\n"
        "v_mfma_f32_32x32x16_bf16 a[16:31], a[156:159], a[76:79], a[16:31]\n"
        "ds_read_b64_tr_b16 a[60:61], v7 offset:2048\n"
        "ds_read_b64_tr_b16 a[62:63], v7 offset:3072\n"
        "v_mul_f32 v167, v247, v227\n"
        "s_waitcnt vmcnt(16)\n"
        "v_lshlrev_b32 v184, 16, v184\n"
        "s_waitcnt vmcnt(15)\n"
        "v_lshlrev_b32 v185, 16, v185\n"
        "v_fma_f32 v246, v184, v230, v231\n"
        "v_fma_f32 v247, v185, v232, v233\n"
        "s_waitcnt lgkmcnt(6)\n"
        "v_mfma_f32_32x32x16_bf16 a[0:15], a[160:163], a[48:51], a[0:15]\n"
        "ds_read_b64_tr_b16 a[64:65], v6 offset:4096\n"
        "ds_read_b64_tr_b16 a[66:67], v6 offset:5120\n"
        "s_waitcnt vmcnt(8)\n"
        "s_mov_b32 s99, 400000\n"
        ".Lp2_%=:\n"
        "v_cmp_eq_u64 vcc, s[96:97], v[32:33]\n"
        "s_cmp_eq_u64 vcc, -1\n"
        "s_cbranch_scc1 .Lq2_%=\n"
        "s_sub_u32 s99, s99, 1\n"
        "s_cmp_eq_u32 s99, 0\n"
        "s_cbranch_scc1 .Lq2_%=\n"
        "s_sleep 1\n"
        "buffer_load_dwordx2 v[32:33], v34, s[56:59], 0 offen offset:1792 sc1\n"
        "s_waitcnt vmcnt(0)\n"
        "s_nop 1\n"
        "s_branch .Lp2_%=\n"
        ".Lq2_%=:\n"
        "s_add_u32 s15, s15, 4096\n"
        "buffer_load_dwordx4 v[16:19], v1, s[56:59], s15 offen sc1\n"
        "buffer_load_dwordx4 v[20:23], v1, s[56:59], s15 offen offset:1024 sc1\n"
        "v_mfma_f32_32x32x16_bf16 a[16:31], a[160:163], a[52:55], a[16:31]\n"
        "ds_read_b64_tr_b16 a[68:69], v7 offset:4096\n"
        "ds_read_b64_tr_b16 a[70:71], v7 offset:5120\n"
        "buffer_load_dwordx4 v[24:27], v1, s[56:59], s15 offen offset:2048 sc1\n"
        "buffer_load_dwordx4 v[28:31], v1, s[56:59], s15 offen offset:3072 sc1\n"
        "v_mul_f32 v168, v246, v227\n"
        "v_mul_f32 v169, v247, v227\n"
        "v_lshlrev_b32 v186, 16, v186\n"
        "v_lshlrev_b32 v187, 16, v187\n"
        "v_fma_f32 v246, v186, v234, v235\n"
        "s_waitcnt lgkmcnt(4)\n"
        "v_mfma_f32_32x32x16_bf16 a[0:15], a[164:167], a[56:59], a[0:15]\n"
        "ds_read_b64_tr_b16 a[72:73], v6 offset:6144\n"
        "ds_read_b64_tr_b16 a[74:75], v6 offset:7168\n"
        "v_fma_f32 v247, v187, v236, v237\n"
        "v_mul_f32 v170, v246, v227\n"
        "v_mul_f32 v171, v247, v227\n"
        "v_lshlrev_b32 v188, 16, v188\n"
        "v_lshlrev_b32 v189, 16, v189\n"
        "v_fma_f32 v246, v188, v238, v239\n"
        "v_mfma_f32_32x32x16_bf16 a[16:31], a[164:167], a[60:63], a[16:31]\n"
        "ds_read_b64_tr_b16 a[76:77], v7 offset:6144\n"
        "ds_read_b64_tr_b16 a[78:79], v7 offset:7168\n"
        "v_fma_f32 v247, v189, v240, v241\n"
        "v_mul_f32 v172, v246, v227\n"
        "v_mul_f32 v173, v247, v227\n"
        "v_lshlrev_b32 v190, 16, v190\n"
        "v_lshlrev_b32 v191, 16, v191\n"
        "v_fma_f32 v246, v190, v242, v243\n"
        "v_fma_f32 v247, v191, v244, v245\n"
        "s_waitcnt lgkmcnt(4)\n"
        "v_mfma_f32_32x32x16_bf16 a[0:15], a[168:171], a[64:67], a[0:15]\n"
        "s_waitcnt lgkmcnt(0)\n"
        "s_barrier\n"
        "v_accvgpr_read_b32 v254, a107\n"
        "buffer_store_dwordx2 v[14:15], v254, s[56:59], 0 offen offset:3584 sc1\n"
        "v_add_u32 v6, 4294836224, v6\n"
        "ds_read_b64_tr_b16 a[48:49], v6\n"
        "ds_read_b64_tr_b16 a[50:51], v6 offset:1024\n"
        "s_add_u32 s12, s12, 57344\n"
        "s_add_u32 m0, s72, 131072\n"
        "s_nop 0\n"
        "buffer_load_dwordx4 v8, s[52:55], s12 offen lds\n"
        "v_mfma_f32_32x32x16_bf16 a[16:31], a[168:171], a[68:71], a[16:31]\n"
        "v_add_u32 v7, 4294836224, v7\n"
        "ds_read_b64_tr_b16 a[52:53], v7\n"
        "ds_read_b64_tr_b16 a[54:55], v7 offset:1024\n"
        "buffer_load_dwordx4 v9, s[52:55], s12 offen offset:1024 lds\n"
        "buffer_load_dwordx4 v10, s[52:55], s12 offen offset:2048 lds\n"
        "v_mul_f32 v174, v246, v227\n"
        "v_mul_f32 v175, v247, v227\n"
        "v_mfma_f32_32x32x16_bf16 a[0:15], a[172:175], a[72:75], a[0:15]\n"
        "ds_read_b64_tr_b16 a[56:57], v6 offset:2048\n"
        "ds_read_b64_tr_b16 a[58:59], v6 offset:3072\n"
        "buffer_load_dwordx4 v11, s[52:55], s12 offen offset:3072 lds\n"
        "v_mfma_f32_32x32x16_bf16 a[16:31], a[172:175], a[76:79], a[16:31]\n"
        "ds_read_b64_tr_b16 a[60:61], v7 offset:2048\n"
        "ds_read_b64_tr_b16 a[62:63], v7 offset:3072\n"
        "s_waitcnt lgkmcnt(4)\n"
        "v_mfma_f32_32x32x16_bf16 a[0:15], a[176:179], a[48:51], a[0:15]\n"
        "ds_read_b64_tr_b16 a[64:65], v6 offset:4096\n"
        "ds_read_b64_tr_b16 a[66:67], v6 offset:5120\n"
        "v_mfma_f32_32x32x16_bf16 a[16:31], a[176:179], a[52:55], a[16:31]\n"
        "ds_read_b64_tr_b16 a[68:69], v7 offset:4096\n"
        "ds_read_b64_tr_b16 a[70:71], v7 offset:5120\n"
        "s_waitcnt lgkmcnt(4)\n"
        "v_mfma_f32_32x32x16_bf16 a[0:15], a[180:183], a[56:59], a[0:15]\n"
        "ds_read_b64_tr_b16 a[72:73], v6 offset:6144\n"
        "ds_read_b64_tr_b16 a[74:75], v6 offset:7168\n"
        "v_mfma_f32_32x32x16_bf16 a[16:31], a[180:183], a[60:63], a[16:31]\n"
        "ds_read_b64_tr_b16 a[76:77], v7 offset:6144\n"
        "ds_read_b64_tr_b16 a[78:79], v7 offset:7168\n"
        "s_waitcnt lgkmcnt(4)\n"
        "v_mfma_f32_32x32x16_bf16 a[0:15], a[184:187], a[64:67], a[0:15]\n"
        "s_waitcnt lgkmcnt(0)\n"
        "s_barrier\n"
        "ds_read_b64_tr_b16 a[48:49], v6 offset:16384\n"
        "ds_read_b64_tr_b16 a[50:51], v6 offset:17408\n"
        "s_add_u32 s13, s13, 57344\n"
        "s_add_u32 m0, s72, 0\n"
        "s_nop 0\n"
        "buffer_load_dwordx4 v8, s[52:55], s13 offen lds\n"
        "buffer_load_dwordx4 v9, s[52:55], s13 offen offset:1024 lds\n"
        "v_mfma_f32_32x32x16_bf16 a[16:31], a[184:187], a[68:71], a[16:31]\n"
        "ds_read_b64_tr_b16 a[52:53], v7 offset:16384\n"
        "ds_read_b64_tr_b16 a[54:55], v7 offset:17408\n"
        "buffer_load_dwordx4 v10, s[52:55], s13 offen offset:2048 lds\n"
        "v_mfma_f32_32x32x16_bf16 a[0:15], a[188:191], a[72:75], a[0:15]\n"
        "ds_read_b64_tr_b16 a[56:57], v6 offset:18432\n"
        "ds_read_b64_tr_b16 a[58:59], v6 offset:19456\n"
        "buffer_load_dwordx4 v11, s[52:55], s13 offen offset:3072 lds\n"
        "v_mfma_f32_32x32x16_bf16 a[16:31], a[188:191], a[76:79], a[16:31]\n"
        "ds_read_b64_tr_b16 a[60:61], v7 offset:18432\n"
        "ds_read_b64_tr_b16 a[62:63], v7 offset:19456\n"
        "s_waitcnt lgkmcnt(4)\n"
        "v_mfma_f32_32x32x16_bf16 a[0:15], a[192:195], a[48:51], a[0:15]\n"
        "ds_read_b64_tr_b16 a[64:65], v6 offset:20480\n"
        "ds_read_b64_tr_b16 a[66:67], v6 offset:21504\n"
        "v_mfma_f32_32x32x16_bf16 a[16:31], a[192:195], a[52:55], a[16:31]\n"
        "ds_read_b64_tr_b16 a[68:69], v7 offset:20480\n"
        "ds_read_b64_tr_b16 a[70:71], v7 offset:21504\n"
        "s_waitcnt lgkmcnt(4)\n"
        "v_mfma_f32_32x32x16_bf16 a[0:15], a[196:199], a[56:59], a[0:15]\n"
        "ds_read_b64_tr_b16 a[72:73], v6 offset:22528\n"
        "ds_read_b64_tr_b16 a[74:75], v6 offset:23552\n"
        "v_mfma_f32_32x32x16_bf16 a[16:31], a[196:199], a[60:63], a[16:31]\n"
        "ds_read_b64_tr_b16 a[76:77], v7 offset:22528\n"
        "ds_read_b64_tr_b16 a[78:79], v7 offset:23552\n"
        "s_waitcnt lgkmcnt(4)\n"
        "v_mfma_f32_32x32x16_bf16 a[0:15], a[200:203], a[64:67], a[0:15]\n"
        "s_waitcnt lgkmcnt(0)\n"
        "s_barrier\n"
        "ds_read_b64_tr_b16 a[48:49], v6 offset:32768\n"
        "ds_read_b64_tr_b16 a[50:51], v6 offset:33792\n"
        "s_add_u32 s14, s14, 57344\n"
        "s_add_u32 m0, s72, 16384\n"
        "s_nop 0\n"
        "buffer_load_dwordx4 v8, s[52:55], s14 offen lds\n"
        "buffer_load_dwordx4 v9, s[52:55], s14 offen offset:1024 lds\n"
        "v_mfma_f32_32x32x16_bf16 a[16:31], a[200:203], a[68:71], a[16:31]\n"
        "ds_read_b64_tr_b16 a[52:53], v7 offset:32768\n"
        "ds_read_b64_tr_b16 a[54:55], v7 offset:33792\n"
        "buffer_load_dwordx4 v10, s[52:55], s14 offen offset:2048 lds\n"
        "v_mfma_f32_32x32x16_bf16 a[0:15], a[204:207], a[72:75], a[0:15]\n"
        "ds_read_b64_tr_b16 a[56:57], v6 offset:34816\n"
        "ds_read_b64_tr_b16 a[58:59], v6 offset:35840\n"
        "buffer_load_dwordx4 v11, s[52:55], s14 offen offset:3072 lds\n"
        "v_mfma_f32_32x32x16_bf16 a[16:31], a[204:207], a[76:79], a[16:31]\n"
        "ds_read_b64_tr_b16 a[60:61], v7 offset:34816\n"
        "ds_read_b64_tr_b16 a[62:63], v7 offset:35840\n"
        "s_waitcnt lgkmcnt(4)\n"
        "v_mfma_f32_32x32x16_bf16 a[0:15], a[208:211], a[48:51], a[0:15]\n"
        "ds_read_b64_tr_b16 a[64:65], v6 offset:36864\n"
        "ds_read_b64_tr_b16 a[66:67], v6 offset:37888\n"
        "v_mfma_f32_32x32x16_bf16 a[16:31], a[208:211], a[52:55], a[16:31]\n"
        "ds_read_b64_tr_b16 a[68:69], v7 offset:36864\n"
        "ds_read_b64_tr_b16 a[70:71], v7 offset:37888\n"
        "s_waitcnt lgkmcnt(4)\n"
        "v_mfma_f32_32x32x16_bf16 a[0:15], a[212:215], a[56:59], a[0:15]\n"
        "ds_read_b64_tr_b16 a[72:73], v6 offset:38912\n"
        "ds_read_b64_tr_b16 a[74:75], v6 offset:39936\n"
        "v_mfma_f32_32x32x16_bf16 a[16:31], a[212:215], a[60:63], a[16:31]\n"
        "ds_read_b64_tr_b16 a[76:77], v7 offset:38912\n"
        "ds_read_b64_tr_b16 a[78:79], v7 offset:39936\n"
        "s_waitcnt lgkmcnt(4)\n"
        "v_mfma_f32_32x32x16_bf16 a[0:15], a[216:219], a[64:67], a[0:15]\n"
        "s_waitcnt lgkmcnt(0)\n"
        "s_barrier\n"
        "ds_read_b64_tr_b16 a[48:49], v6 offset:49152\n"
        "ds_read_b64_tr_b16 a[50:51], v6 offset:50176\n"
        "v_accvgpr_read_b32 v248, a88\n"
        "v_accvgpr_read_b32 v249, a89\n"
        "v_add_u32 v250, 12288, v248\n"
        "v_add_u32 v251, 18432, v249\n"
        "v_add_u32 v249, 6144, v249\n"
        "s_add_u32 m0, s72, 32768\n"
        "s_nop 0\n"
        "buffer_load_dwordx4 v248, s[44:47], s29 offen lds\n"
        "buffer_load_dwordx4 v249, s[44:47], s29 offen offset:1024 lds\n"
        "v_mfma_f32_32x32x16_bf16 a[16:31], a[216:219], a[68:71], a[16:31]\n"
        "ds_read_b64_tr_b16 a[52:53], v7 offset:49152\n"
        "ds_read_b64_tr_b16 a[54:55], v7 offset:50176\n"
        "buffer_load_dwordx4 v250, s[44:47], s29 offen offset:2048 lds\n"
        "v_mfma_f32_32x32x16_bf16 a[0:15], a[220:223], a[72:75], a[0:15]\n"
        "ds_read_b64_tr_b16 a[56:57], v6 offset:51200\n"
        "ds_read_b64_tr_b16 a[58:59], v6 offset:52224\n"
        "buffer_load_dwordx4 v251, s[44:47], s29 offen offset:3072 lds\n"
        "v_mfma_f32_32x32x16_bf16 a[16:31], a[220:223], a[76:79], a[16:31]\n"
        "ds_read_b64_tr_b16 a[60:61], v7 offset:51200\n"
        "ds_read_b64_tr_b16 a[62:63], v7 offset:52224\n"
        "s_waitcnt lgkmcnt(4)\n"
        "v_mfma_f32_32x32x16_bf16 a[0:15], a[224:227], a[48:51], a[0:15]\n"
        "ds_read_b64_tr_b16 a[64:65], v6 offset:53248\n"
        "ds_read_b64_tr_b16 a[66:67], v6 offset:54272\n"
        "v_accvgpr_read_b32 v218, a108\n"
        "buffer_load_dwordx2 v[216:217], v218, s[56:59], 0 offen offset:3584 sc1\n"
        "v_mfma_f32_32x32x16_bf16 a[16:31], a[224:227], a[52:55], a[16:31]\n"
        "ds_read_b64_tr_b16 a[68:69], v7 offset:53248\n"
        "ds_read_b64_tr_b16 a[70:71], v7 offset:54272\n"
        "s_add_u32 s16, s16, 4096\n"
        "buffer_load_dwordx4 v[32:35], v1, s[56:59], s16 offen sc1\n"
        "buffer_load_dwordx4 v[36:39], v1, s[56:59], s16 offen offset:1024 sc1\n"
        "s_waitcnt lgkmcnt(4)\n"
        "v_mfma_f32_32x32x16_bf16 a[0:15], a[228:231], a[56:59], a[0:15]\n"
        "ds_read_b64_tr_b16 a[72:73], v6 offset:55296\n"
        "ds_read_b64_tr_b16 a[74:75], v6 offset:56320\n"
        "buffer_load_dwordx4 v[40:43], v1, s[56:59], s16 offen offset:2048 sc1\n"
        "buffer_load_dwordx4 v[44:47], v1, s[56:59], s16 offen offset:3072 sc1\n"
        "v_mfma_f32_32x32x16_bf16 a[16:31], a[228:231], a[60:63], a[16:31]\n"
        "ds_read_b64_tr_b16 a[76:77], v7 offset:55296\n"
        "ds_read_b64_tr_b16 a[78:79], v7 offset:56320\n"
        "s_add_u32 s17, s17, 4096\n"
        "buffer_load_dwordx4 v[48:51], v1, s[56:59], s17 offen sc1\n"
        "buffer_load_dwordx4 v[52:55], v1, s[56:59], s17 offen offset:1024 sc1\n"
        "s_waitcnt lgkmcnt(4)\n"
        "v_mfma_f32_32x32x16_bf16 a[0:15], a[232:235], a[64:67], a[0:15]\n"
        "s_waitcnt lgkmcnt(0)\n"
        "s_barrier\n"
        "v_add_u32 v6, 65536, v6\n"
        "ds_read_b64_tr_b16 a[48:49], v6\n"
        "ds_read_b64_tr_b16 a[50:51], v6 offset:1024\n"
        "buffer_load_dwordx4 v[56:59], v1, s[56:59], s17 offen offset:2048 sc1\n"
        "buffer_load_dwordx4 v[60:63], v1, s[56:59], s17 offen offset:3072 sc1\n"
        "s_add_u32 m0, s72, 49152\n"
        "v_mfma_f32_32x32x16_bf16 a[16:31], a[232:235], a[68:71], a[16:31]\n"
        "v_add_u32 v7, 65536, v7\n"
        "ds_read_b64_tr_b16 a[52:53], v7\n"
        "ds_read_b64_tr_b16 a[54:55], v7 offset:1024\n"
        "s_add_u32 s18, s18, 4096\n"
        "buffer_load_dwordx4 v[64:67], v1, s[56:59], s18 offen sc1\n"
        "buffer_load_dwordx4 v[68:71], v1, s[56:59], s18 offen offset:1024 sc1\n"
        "v_mfma_f32_32x32x16_bf16 a[0:15], a[236:239], a[72:75], a[0:15]\n"
        "ds_read_b64_tr_b16 a[56:57], v6 offset:2048\n"
        "ds_read_b64_tr_b16 a[58:59], v6 offset:3072\n"
        "buffer_load_dwordx4 v[72:75], v1, s[56:59], s18 offen offset:2048 sc1\n"
        "buffer_load_dwordx4 v[76:79], v1, s[56:59], s18 offen offset:3072 sc1\n"
        "v_mfma_f32_32x32x16_bf16 a[16:31], a[236:239], a[76:79], a[16:31]\n"
        "ds_read_b64_tr_b16 a[60:61], v7 offset:2048\n"
        "ds_read_b64_tr_b16 a[62:63], v7 offset:3072\n"
        "s_add_u32 s19, s19, 4096\n"
        "buffer_load_dwordx4 v[80:83], v1, s[56:59], s19 offen sc1\n"
        "buffer_load_dwordx4 v[84:87], v1, s[56:59], s19 offen offset:1024 sc1\n"
        "s_waitcnt vmcnt(35) lgkmcnt(4)\n"
        "v_mfma_f32_32x32x16_bf16 a[0:15], v[16:19], a[48:51], a[0:15]\n"
        "ds_read_b64_tr_b16 a[64:65], v6 offset:4096\n"
        "ds_read_b64_tr_b16 a[66:67], v6 offset:5120\n"
        "buffer_load_dwordx4 v[88:91], v1, s[56:59], s19 offen offset:2048 sc1\n"
        "buffer_load_dwordx4 v[92:95], v1, s[56:59], s19 offen offset:3072 sc1\n"
        "v_mfma_f32_32x32x16_bf16 a[16:31], v[16:19], a[52:55], a[16:31]\n"
        "ds_read_b64_tr_b16 a[68:69], v7 offset:4096\n"
        "ds_read_b64_tr_b16 a[70:71], v7 offset:5120\n"
        "s_add_u32 s20, s20, 4096\n"
        "buffer_load_dwordx4 v[96:99], v1, s[56:59], s20 offen sc1\n"
        "buffer_load_dwordx4 v[100:103], v1, s[56:59], s20 offen offset:1024 sc1\n"
        "s_waitcnt vmcnt(38) lgkmcnt(4)\n"
        "v_mfma_f32_32x32x16_bf16 a[0:15], v[20:23], a[56:59], a[0:15]\n"
        "ds_read_b64_tr_b16 a[72:73], v6 offset:6144\n"
        "ds_read_b64_tr_b16 a[74:75], v6 offset:7168\n"
        "buffer_load_dwordx4 v[104:107], v1, s[56:59], s20 offen offset:2048 sc1\n"
        "buffer_load_dwordx4 v[108:111], v1, s[56:59], s20 offen offset:3072 sc1\n"
        "v_mfma_f32_32x32x16_bf16 a[16:31], v[20:23], a[60:63], a[16:31]\n"
        "ds_read_b64_tr_b16 a[76:77], v7 offset:6144\n"
        "ds_read_b64_tr_b16 a[78:79], v7 offset:7168\n"
        "s_add_u32 s21, s21, 4096\n"
        "buffer_load_dwordx4 v[112:115], v1, s[56:59], s21 offen sc1\n"
        "buffer_load_dwordx4 v[116:119], v1, s[56:59], s21 offen offset:1024 sc1\n"
        "s_waitcnt vmcnt(41) lgkmcnt(4)\n"
        "v_mfma_f32_32x32x16_bf16 a[0:15], v[24:27], a[64:67], a[0:15]\n"
        "s_waitcnt lgkmcnt(0)\n"
        "s_barrier\n"
        "ds_read_b64_tr_b16 a[48:49], v6 offset:16384\n"
        "ds_read_b64_tr_b16 a[50:51], v6 offset:17408\n"
        "buffer_load_dwordx4 v[120:123], v1, s[56:59], s21 offen offset:2048 sc1\n"
        "buffer_load_dwordx4 v[124:127], v1, s[56:59], s21 offen offset:3072 sc1\n"
        "v_mfma_f32_32x32x16_bf16 a[16:31], v[24:27], a[68:71], a[16:31]\n"
        "ds_read_b64_tr_b16 a[52:53], v7 offset:16384\n"
        "ds_read_b64_tr_b16 a[54:55], v7 offset:17408\n"
        "buffer_load_dwordx4 v248, s[44:47], s30 offen lds\n"
        "buffer_load_dwordx4 v249, s[44:47], s30 offen offset:1024 lds\n"
        "s_waitcnt vmcnt(44)\n"
        "v_mfma_f32_32x32x16_bf16 a[0:15], v[28:31], a[72:75], a[0:15]\n"
        "ds_read_b64_tr_b16 a[56:57], v6 offset:18432\n"
        "ds_read_b64_tr_b16 a[58:59], v6 offset:19456\n"
        "buffer_load_dwordx4 v250, s[44:47], s30 offen offset:2048 lds\n"
        "v_mfma_f32_32x32x16_bf16 a[16:31], v[28:31], a[76:79], a[16:31]\n"
        "ds_read_b64_tr_b16 a[60:61], v7 offset:18432\n"
        "ds_read_b64_tr_b16 a[62:63], v7 offset:19456\n"
        "buffer_load_dwordx4 v251, s[44:47], s30 offen offset:3072 lds\n"
        "s_add_u32 m0, s72, 65536\n"
        "s_waitcnt vmcnt(27) lgkmcnt(4)\n"
        "v_mfma_f32_32x32x16_bf16 a[0:15], v[32:35], a[48:51], a[0:15]\n"
        "ds_read_b64_tr_b16 a[64:65], v6 offset:20480\n"
        "ds_read_b64_tr_b16 a[66:67], v6 offset:21504\n"
        "s_mov_b32 s99, 400000\n"
        ".Lp3_%=:\n"
        "v_cmp_eq_u64 vcc, s[96:97], v[216:217]\n"
        "s_cmp_eq_u64 vcc, -1\n"
        "s_cbranch_scc1 .Lq3_%=\n"
        "s_sub_u32 s99, s99, 1\n"
        "s_cmp_eq_u32 s99, 0\n"
        "s_cbranch_scc1 .Lq3_%=\n"
        "s_sleep 1\n"
        "buffer_load_dwordx2 v[216:217], v218, s[56:59], 0 offen offset:3584 sc1\n"
        "s_waitcnt vmcnt(0)\n"
        "s_nop 1\n"
        "s_branch .Lp3_%=\n"
        ".Lq3_%=:\n"
        "buffer_load_dwordx4 a[128:131], v1, s[56:59], s22 offen sc1\n"
        "buffer_load_dwordx4 a[132:135], v1, s[56:59], s22 offen offset:1024 sc1\n"
        "v_mfma_f32_32x32x16_bf16 a[16:31], v[32:35], a[52:55], a[16:31]\n"
        "ds_read_b64_tr_b16 a[68:69], v7 offset:20480\n"
        "ds_read_b64_tr_b16 a[70:71], v7 offset:21504\n"
        "buffer_load_dwordx4 a[136:139], v1, s[56:59], s22 offen offset:2048 sc1\n"
        "buffer_load_dwordx4 a[140:143], v1, s[56:59], s22 offen offset:3072 sc1\n"
        "s_waitcnt vmcnt(30) lgkmcnt(4)\n"
        "v_mfma_f32_32x32x16_bf16 a[0:15], v[36:39], a[56:59], a[0:15]\n"
        "ds_read_b64_tr_b16 a[72:73], v6 offset:22528\n"
        "ds_read_b64_tr_b16 a[74:75], v6 offset:23552\n"
        "buffer_load_dwordx4 v248, s[44:47], s31 offen lds\n"
        "v_mfma_f32_32x32x16_bf16 a[16:31], v[36:39], a[60:63], a[16:31]\n"
        "ds_read_b64_tr_b16 a[76:77], v7 offset:22528\n"
        "ds_read_b64_tr_b16 a[78:79], v7 offset:23552\n"
        "buffer_load_dwordx4 v249, s[44:47], s31 offen offset:1024 lds\n"
        "s_waitcnt vmcnt(31) lgkmcnt(4)\n"
        "v_mfma_f32_32x32x16_bf16 a[0:15], v[40:43], a[64:67], a[0:15]\n"
        "s_waitcnt lgkmcnt(0)\n"
        "s_barrier\n"
        "ds_read_b64_tr_b16 a[48:49], v6 offset:32768\n"
        "ds_read_b64_tr_b16 a[50:51], v6 offset:33792\n"
        "buffer_load_dwordx4 v250, s[44:47], s31 offen offset:2048 lds\n"
        "v_mfma_f32_32x32x16_bf16 a[16:31], v[40:43], a[68:71], a[16:31]\n"
        "ds_read_b64_tr_b16 a[52:53], v7 offset:32768\n"
        "ds_read_b64_tr_b16 a[54:55], v7 offset:33792\n"
        "buffer_load_dwordx4 v251, s[44:47], s31 offen offset:3072 lds\n"
        "s_add_u32 m0, s72, 81920\n"
        "s_nop 0\n"
        "buffer_load_dwordx4 v248, s[44:47], s32 offen lds\n"
        "s_waitcnt vmcnt(33)\n"
        "v_mfma_f32_32x32x16_bf16 a[0:15], v[44:47], a[72:75], a[0:15]\n"
        "ds_read_b64_tr_b16 a[56:57], v6 offset:34816\n"
        "ds_read_b64_tr_b16 a[58:59], v6 offset:35840\n"
        "buffer_load_dwordx4 v249, s[44:47], s32 offen offset:1024 lds\n"
        "v_mfma_f32_32x32x16_bf16 a[16:31], v[44:47], a[76:79], a[16:31]\n"
        "ds_read_b64_tr_b16 a[60:61], v7 offset:34816\n"
        "ds_read_b64_tr_b16 a[62:63], v7 offset:35840\n"
        "buffer_load_dwordx4 v250, s[44:47], s32 offen offset:2048 lds\n"
        "s_waitcnt vmcnt(34) lgkmcnt(4)\n"
        "v_mfma_f32_32x32x16_bf16 a[0:15], v[48:51], a[48:51], a[0:15]\n"
        "ds_read_b64_tr_b16 a[64:65], v6 offset:36864\n"
        "ds_read_b64_tr_b16 a[66:67], v6 offset:37888\n"
        "buffer_load_dwordx4 v251, s[44:47], s32 offen offset:3072 lds\n"
        "v_mfma_f32_32x32x16_bf16 a[16:31], v[48:51], a[52:55], a[16:31]\n"
        "ds_read_b64_tr_b16 a[68:69], v7 offset:36864\n"
        "ds_read_b64_tr_b16 a[70:71], v7 offset:37888\n"
        "s_waitcnt vmcnt(34) lgkmcnt(4)\n"
        "v_mfma_f32_32x32x16_bf16 a[0:15], v[52:55], a[56:59], a[0:15]\n"
        "ds_read_b64_tr_b16 a[72:73], v6 offset:38912\n"
        "ds_read_b64_tr_b16 a[74:75], v6 offset:39936\n"
        "v_mfma_f32_32x32x16_bf16 a[16:31], v[52:55], a[60:63], a[16:31]\n"
        "ds_read_b64_tr_b16 a[76:77], v7 offset:38912\n"
        "ds_read_b64_tr_b16 a[78:79], v7 offset:39936\n"
        "s_waitcnt vmcnt(33) lgkmcnt(4)\n"
        "v_mfma_f32_32x32x16_bf16 a[0:15], v[56:59], a[64:67], a[0:15]\n"
        "s_waitcnt lgkmcnt(0)\n"
        "s_barrier\n"
        "ds_read_b64_tr_b16 a[48:49], v6 offset:49152\n"
        "ds_read_b64_tr_b16 a[50:51], v6 offset:50176\n"
        "s_add_u32 m0, s72, 98304\n"
        "s_nop 0\n"
        "buffer_load_dwordx4 v248, s[44:47], s33 offen lds\n"
        "buffer_load_dwordx4 v249, s[44:47], s33 offen offset:1024 lds\n"
        "v_mfma_f32_32x32x16_bf16 a[16:31], v[56:59], a[68:71], a[16:31]\n"
        "ds_read_b64_tr_b16 a[52:53], v7 offset:49152\n"
        "ds_read_b64_tr_b16 a[54:55], v7 offset:50176\n"
        "buffer_load_dwordx4 v250, s[44:47], s33 offen offset:2048 lds\n"
        "s_waitcnt vmcnt(35)\n"
        "v_mfma_f32_32x32x16_bf16 a[0:15], v[60:63], a[72:75], a[0:15]\n"
        "ds_read_b64_tr_b16 a[56:57], v6 offset:51200\n"
        "ds_read_b64_tr_b16 a[58:59], v6 offset:52224\n"
        "buffer_load_dwordx4 v251, s[44:47], s33 offen offset:3072 lds\n"
        "v_mfma_f32_32x32x16_bf16 a[16:31], v[60:63], a[76:79], a[16:31]\n"
        "ds_read_b64_tr_b16 a[60:61], v7 offset:51200\n"
        "ds_read_b64_tr_b16 a[62:63], v7 offset:52224\n"
        "s_waitcnt vmcnt(35) lgkmcnt(4)\n"
        "v_mfma_f32_32x32x16_bf16 a[0:15], v[64:67], a[48:51], a[0:15]\n"
        "ds_read_b64_tr_b16 a[64:65], v6 offset:53248\n"
        "ds_read_b64_tr_b16 a[66:67], v6 offset:54272\n"
        "v_mfma_f32_32x32x16_bf16 a[16:31], v[64:67], a[52:55], a[16:31]\n"
        "ds_read_b64_tr_b16 a[68:69], v7 offset:53248\n"
        "ds_read_b64_tr_b16 a[70:71], v7 offset:54272\n"
        "s_waitcnt vmcnt(34) lgkmcnt(4)\n"
        "v_mfma_f32_32x32x16_bf16 a[0:15], v[68:71], a[56:59], a[0:15]\n"
        "ds_read_b64_tr_b16 a[72:73], v6 offset:55296\n"
        "ds_read_b64_tr_b16 a[74:75], v6 offset:56320\n"
        "v_mfma_f32_32x32x16_bf16 a[16:31], v[68:71], a[60:63], a[16:31]\n"
        "ds_read_b64_tr_b16 a[76:77], v7 offset:55296\n"
        "ds_read_b64_tr_b16 a[78:79], v7 offset:56320\n"
        "s_waitcnt vmcnt(33) lgkmcnt(4)\n"
        "v_mfma_f32_32x32x16_bf16 a[0:15], v[72:75], a[64:67], a[0:15]\n"
        "s_waitcnt lgkmcnt(0)\n"
        "s_barrier\n"
        "v_add_u32 v6, 65536, v6\n"
        "ds_read_b64_tr_b16 a[48:49], v6\n"
        "ds_read_b64_tr_b16 a[50:51], v6 offset:1024\n"
        "s_add_u32 m0, s72, 114688\n"
        "s_nop 0\n"
        "buffer_load_dwordx4 v248, s[44:47], s34 offen lds\n"
        "buffer_load_dwordx4 v249, s[44:47], s34 offen offset:1024 lds\n"
        "v_mfma_f32_32x32x16_bf16 a[16:31], v[72:75], a[68:71], a[16:31]\n"
        "v_add_u32 v7, 65536, v7\n"
        "ds_read_b64_tr_b16 a[52:53], v7\n"
        "ds_read_b64_tr_b16 a[54:55], v7 offset:1024\n"
        "buffer_load_dwordx4 v250, s[44:47], s34 offen offset:2048 lds\n"
        "s_waitcnt vmcnt(35)\n"
        "v_mfma_f32_32x32x16_bf16 a[0:15], v[76:79], a[72:75], a[0:15]\n"
        "ds_read_b64_tr_b16 a[56:57], v6 offset:2048\n"
        "ds_read_b64_tr_b16 a[58:59], v6 offset:3072\n"
        "buffer_load_dwordx4 v251, s[44:47], s34 offen offset:3072 lds\n"
        "v_mfma_f32_32x32x16_bf16 a[16:31], v[76:79], a[76:79], a[16:31]\n"
        "ds_read_b64_tr_b16 a[60:61], v7 offset:2048\n"
        "ds_read_b64_tr_b16 a[62:63], v7 offset:3072\n"
        "s_waitcnt vmcnt(35) lgkmcnt(4)\n"
        "v_mfma_f32_32x32x16_bf16 a[0:15], v[80:83], a[48:51], a[0:15]\n"
        "ds_read_b64_tr_b16 a[64:65], v6 offset:4096\n"
        "ds_read_b64_tr_b16 a[66:67], v6 offset:5120\n"
        "v_mfma_f32_32x32x16_bf16 a[16:31], v[80:83], a[52:55], a[16:31]\n"
        "ds_read_b64_tr_b16 a[68:69], v7 offset:4096\n"
        "ds_read_b64_tr_b16 a[70:71], v7 offset:5120\n"
        "s_waitcnt vmcnt(34) lgkmcnt(4)\n"
        "v_mfma_f32_32x32x16_bf16 a[0:15], v[84:87], a[56:59], a[0:15]\n"
        "ds_read_b64_tr_b16 a[72:73], v6 offset:6144\n"
        "ds_read_b64_tr_b16 a[74:75], v6 offset:7168\n"
        "v_mfma_f32_32x32x16_bf16 a[16:31], v[84:87], a[60:63], a[16:31]\n"
        "ds_read_b64_tr_b16 a[76:77], v7 offset:6144\n"
        "ds_read_b64_tr_b16 a[78:79], v7 offset:7168\n"
        "s_waitcnt vmcnt(33) lgkmcnt(4)\n"
        "v_mfma_f32_32x32x16_bf16 a[0:15], v[88:91], a[64:67], a[0:15]\n"
        "s_waitcnt lgkmcnt(0)\n"
        "s_barrier\n"
        "v_add_u32 v6, 4294836224, v6\n"
        "ds_read_b64_tr_b16 a[48:49], v6\n"
        "ds_read_b64_tr_b16 a[50:51], v6 offset:1024\n"
        "s_add_u32 m0, s72, 131072\n"
        "s_nop 0\n"
        "buffer_load_dwordx4 v248, s[44:47], s35 offen lds\n"
        "buffer_load_dwordx4 v249, s[44:47], s35 offen offset:1024 lds\n"
        "v_mfma_f32_32x32x16_bf16 a[16:31], v[88:91], a[68:71], a[16:31]\n"
        "v_add_u32 v7, 4294836224, v7\n"
        "ds_read_b64_tr_b16 a[52:53], v7\n"
        "ds_read_b64_tr_b16 a[54:55], v7 offset:1024\n"
        "buffer_load_dwordx4 v250, s[44:47], s35 offen offset:2048 lds\n"
        "s_waitcnt vmcnt(35)\n"
        "v_mfma_f32_32x32x16_bf16 a[0:15], v[92:95], a[72:75], a[0:15]\n"
        "ds_read_b64_tr_b16 a[56:57], v6 offset:2048\n"
        "ds_read_b64_tr_b16 a[58:59], v6 offset:3072\n"
        "buffer_load_dwordx4 v251, s[44:47], s35 offen offset:3072 lds\n"
        "v_mfma_f32_32x32x16_bf16 a[16:31], v[92:95], a[76:79], a[16:31]\n"
        "ds_read_b64_tr_b16 a[60:61], v7 offset:2048\n"
        "ds_read_b64_tr_b16 a[62:63], v7 offset:3072\n"
        "s_waitcnt vmcnt(35) lgkmcnt(4)\n"
        "v_mfma_f32_32x32x16_bf16 a[0:15], v[96:99], a[48:51], a[0:15]\n"
        "ds_read_b64_tr_b16 a[64:65], v6 offset:4096\n"
        "ds_read_b64_tr_b16 a[66:67], v6 offset:5120\n"
        "v_mfma_f32_32x32x16_bf16 a[16:31], v[96:99], a[52:55], a[16:31]\n"
        "ds_read_b64_tr_b16 a[68:69], v7 offset:4096\n"
        "ds_read_b64_tr_b16 a[70:71], v7 offset:5120\n"
        "buffer_load_dwordx4 a[144:147], v1, s[56:59], s23 offen sc1\n"
        "buffer_load_dwordx4 a[148:151], v1, s[56:59], s23 offen offset:1024 sc1\n"
        "s_waitcnt vmcnt(36) lgkmcnt(4)\n"
        "v_mfma_f32_32x32x16_bf16 a[0:15], v[100:103], a[56:59], a[0:15]\n"
        "ds_read_b64_tr_b16 a[72:73], v6 offset:6144\n"
        "ds_read_b64_tr_b16 a[74:75], v6 offset:7168\n"
        "buffer_load_dwordx4 a[152:155], v1, s[56:59], s23 offen offset:2048 sc1\n"
        "buffer_load_dwordx4 a[156:159], v1, s[56:59], s23 offen offset:3072 sc1\n"
        "v_mfma_f32_32x32x16_bf16 a[16:31], v[100:103], a[60:63], a[16:31]\n"
        "ds_read_b64_tr_b16 a[76:77], v7 offset:6144\n"
        "ds_read_b64_tr_b16 a[78:79], v7 offset:7168\n"
        "buffer_load_dwordx4 a[160:163], v1, s[56:59], s24 offen sc1\n"
        "buffer_load_dwordx4 a[164:167], v1, s[56:59], s24 offen offset:1024 sc1\n"
        "s_waitcnt vmcnt(39) lgkmcnt(4)\n"
        "v_mfma_f32_32x32x16_bf16 a[0:15], v[104:107], a[64:67], a[0:15]\n"
        "s_waitcnt lgkmcnt(0)\n"
        "s_barrier\n"
        "ds_read_b64_tr_b16 a[48:49], v6 offset:16384\n"
        "ds_read_b64_tr_b16 a[50:51], v6 offset:17408\n"
        "buffer_load_dwordx4 a[168:171], v1, s[56:59], s24 offen offset:2048 sc1\n"
        "buffer_load_dwordx4 a[172:175], v1, s[56:59], s24 offen offset:3072 sc1\n"
        "v_mfma_f32_32x32x16_bf16 a[16:31], v[104:107], a[68:71], a[16:31]\n"
        "ds_read_b64_tr_b16 a[52:53], v7 offset:16384\n"
        "ds_read_b64_tr_b16 a[54:55], v7 offset:17408\n"
        "buffer_load_dwordx4 a[176:179], v1, s[56:59], s25 offen sc1\n"
        "buffer_load_dwordx4 a[180:183], v1, s[56:59], s25 offen offset:1024 sc1\n"
        "s_waitcnt vmcnt(42)\n"
        "v_mfma_f32_32x32x16_bf16 a[0:15], v[108:111], a[72:75], a[0:15]\n"
        "ds_read_b64_tr_b16 a[56:57], v6 offset:18432\n"
        "ds_read_b64_tr_b16 a[58:59], v6 offset:19456\n"
        "buffer_load_dwordx4 a[184:187], v1, s[56:59], s25 offen offset:2048 sc1\n"
        "buffer_load_dwordx4 a[188:191], v1, s[56:59], s25 offen offset:3072 sc1\n"
        "v_mfma_f32_32x32x16_bf16 a[16:31], v[108:111], a[76:79], a[16:31]\n"
        "ds_read_b64_tr_b16 a[60:61], v7 offset:18432\n"
        "ds_read_b64_tr_b16 a[62:63], v7 offset:19456\n"
        "buffer_load_dwordx4 a[192:195], v1, s[56:59], s26 offen sc1\n"
        "buffer_load_dwordx4 a[196:199], v1, s[56:59], s26 offen offset:1024 sc1\n"
        "s_waitcnt vmcnt(45) lgkmcnt(4)\n"
        "v_mfma_f32_32x32x16_bf16 a[0:15], v[112:115], a[48:51], a[0:15]\n"
        "ds_read_b64_tr_b16 a[64:65], v6 offset:20480\n"
        "ds_read_b64_tr_b16 a[66:67], v6 offset:21504\n"
        "buffer_load_dwordx4 a[200:203], v1, s[56:59], s26 offen offset:2048 sc1\n"
        "buffer_load_dwordx4 a[204:207], v1, s[56:59], s26 offen offset:3072 sc1\n"
        "v_mfma_f32_32x32x16_bf16 a[16:31], v[112:115], a[52:55], a[16:31]\n"
        "ds_read_b64_tr_b16 a[68:69], v7 offset:20480\n"
        "ds_read_b64_tr_b16 a[70:71], v7 offset:21504\n"
        "buffer_load_dwordx4 a[208:211], v1, s[56:59], s27 offen sc1\n"
        "buffer_load_dwordx4 a[212:215], v1, s[56:59], s27 offen offset:1024 sc1\n"
        "s_waitcnt vmcnt(48) lgkmcnt(4)\n"
        "v_mfma_f32_32x32x16_bf16 a[0:15], v[116:119], a[56:59], a[0:15]\n"
        "ds_read_b64_tr_b16 a[72:73], v6 offset:22528\n"
        "ds_read_b64_tr_b16 a[74:75], v6 offset:23552\n"
        "buffer_load_dwordx4 a[216:219], v1, s[56:59], s27 offen offset:2048 sc1\n"
        "buffer_load_dwordx4 a[220:223], v1, s[56:59], s27 offen offset:3072 sc1\n"
        "v_mfma_f32_32x32x16_bf16 a[16:31], v[116:119], a[60:63], a[16:31]\n"
        "ds_read_b64_tr_b16 a[76:77], v7 offset:22528\n"
        "ds_read_b64_tr_b16 a[78:79], v7 offset:23552\n"
        "buffer_load_dwordx4 a[224:227], v1, s[56:59], s28 offen sc1\n"
        "buffer_load_dwordx4 a[228:231], v1, s[56:59], s28 offen offset:1024 sc1\n"
        "s_waitcnt vmcnt(51) lgkmcnt(4)\n"
        "v_mfma_f32_32x32x16_bf16 a[0:15], v[120:123], a[64:67], a[0:15]\n"
        "s_waitcnt lgkmcnt(0)\n"
        "s_barrier\n"
        "v_add_u32 v192, 4294901760, v192\n"
        "ds_read_b128 a[48:51], v192 offset:32768\n"
        "buffer_load_dwordx4 a[232:235], v1, s[56:59], s28 offen offset:2048 sc1\n"
        "buffer_load_dwordx4 a[236:239], v1, s[56:59], s28 offen offset:3072 sc1\n"
        "v_mfma_f32_32x32x16_bf16 a[16:31], v[120:123], a[68:71], a[16:31]\n"
        "v_add_u32 v196, 4294901760, v196\n"
        "ds_read_b128 a[52:55], v196 offset:32768\n"
        "s_waitcnt vmcnt(52)\n"
        "v_mfma_f32_32x32x16_bf16 a[0:15], v[124:127], a[72:75], a[0:15]\n"
        "v_add_u32 v193, 4294901760, v193\n"
        "ds_read_b128 a[56:59], v193 offset:32768\n"
        "v_mfma_f32_32x32x16_bf16 a[16:31], v[124:127], a[76:79], a[16:31]\n"
        "v_add_u32 v197, 4294901760, v197\n"
        "ds_read_b128 a[60:63], v197 offset:32768\n"
        "s_waitcnt vmcnt(47) lgkmcnt(2)\n"
        "v_mfma_f32_32x32x16_bf16 v[160:175], a[128:131], a[48:51], v[160:175]\n"
        "v_add_u32 v194, 4294901760, v194\n"
        "ds_read_b128 a[64:67], v194 offset:32768\n"
        "v_add_u32 v228, s72, v1\n"
        "s_nop 4\n"
        "ds_write_b128 v228, a[16:19]\n"
        "ds_write_b128 v228, a[20:23] offset:1024\n"
        "v_mfma_f32_32x32x16_bf16 a[32:47], a[128:131], a[52:55], 0\n"
        "v_add_u32 v198, 4294901760, v198\n"
        "ds_read_b128 a[68:71], v198 offset:32768\n"
        "v_accvgpr_read_b32 v222, a109\n"
        "v_accvgpr_read_b32 v223, a110\n"
        "buffer_load_dwordx2 v[220:221], v222, s[56:59], 0 offen sc1\n"
        "ds_write_b128 v228, a[24:27] offset:2048\n"
        "s_waitcnt vmcnt(47) lgkmcnt(5)\n"
        "v_mfma_f32_32x32x16_bf16 v[160:175], a[132:135], a[56:59], v[160:175]\n"
        "v_add_u32 v195, 4294901760, v195\n"
        "ds_read_b128 a[72:75], v195 offset:32768\n"
        "ds_write_b128 v228, a[28:31] offset:3072\n"
        "v_mfma_f32_32x32x16_bf16 a[32:47], a[132:135], a[60:63], a[32:47]\n"
        "v_add_u32 v199, 4294901760, v199\n"
        "ds_read_b128 a[76:79], v199 offset:32768\n"
        "s_waitcnt vmcnt(46) lgkmcnt(4)\n"
        "v_mfma_f32_32x32x16_bf16 v[160:175], a[136:139], a[64:67], v[160:175]\n"
        "s_waitcnt lgkmcnt(0)\n"
        "s_barrier\n"
        "ds_read_b128 a[48:51], v192 offset:49152\n"
        "s_xor_b32 s95, s72, 8192\n"
        "v_add_u32 v228, s95, v1\n"
        "v_mfma_f32_32x32x16_bf16 a[32:47], a[136:139], a[68:71], a[32:47]\n"
        "ds_read_b128 a[52:55], v196 offset:49152\n"
        "ds_read_b128 v[128:131], v228\n"
        "ds_read_b128 v[132:135], v228 offset:1024\n"
        "ds_read_b128 v[136:139], v228 offset:2048\n"
        "ds_read_b128 v[140:143], v228 offset:3072\n"
        "v_accvgpr_read_b32 v230, a0\n"
        "s_waitcnt vmcnt(45)\n"
        "v_mfma_f32_32x32x16_bf16 v[160:175], a[140:143], a[72:75], v[160:175]\n"
        "ds_read_b128 a[56:59], v193 offset:49152\n"
        "v_accvgpr_read_b32 v231, a1\n"
        "s_waitcnt lgkmcnt(4)\n"
        "v_add_f32 v230, v230, v128\n"
        "v_add_f32 v231, v231, v129\n"
        "v_mfma_f32_32x32x16_bf16 a[32:47], a[140:143], a[76:79], a[32:47]\n"
        "ds_read_b128 a[60:63], v197 offset:49152\n"
        "v_mul_f32 v232, 0xbfb8aa3b, v230\n"
        "v_mul_f32 v233, 0xbfb8aa3b, v231\n"
        "v_exp_f32 v232, v232\n"
        "v_exp_f32 v233, v233\n"
        "v_add_f32 v232, 1.0, v232\n"
        "s_waitcnt vmcnt(24)\n"
        "v_mfma_f32_32x32x16_bf16 v[160:175], a[144:147], a[48:51], v[160:175]\n"
        "ds_read_b128 a[64:67], v194 offset:49152\n"
        "v_add_f32 v233, 1.0, v233\n"
        "v_rcp_f32 v232, v232\n"
        "v_rcp_f32 v233, v233\n"
        "v_mul_f32 v200, v230, v232\n"
        "v_mfma_f32_32x32x16_bf16 a[32:47], a[144:147], a[52:55], a[32:47]\n"
        "ds_read_b128 a[68:71], v198 offset:49152\n"
        "v_mul_f32 v201, v231, v233\n"
        "v_accvgpr_read_b32 v234, a2\n"
        "v_accvgpr_read_b32 v235, a3\n"
        "v_add_f32 v234, v234, v130\n"
        "v_add_f32 v235, v235, v131\n"
        "s_waitcnt vmcnt(23) lgkmcnt(2)\n"
        "v_mfma_f32_32x32x16_bf16 v[160:175], a[148:151], a[56:59], v[160:175]\n"
        "ds_read_b128 a[72:75], v195 offset:49152\n"
        "v_mul_f32 v236, 0xbfb8aa3b, v234\n"
        "v_mul_f32 v237, 0xbfb8aa3b, v235\n"
        "v_exp_f32 v236, v236\n"
        "v_exp_f32 v237, v237\n"
        "v_mfma_f32_32x32x16_bf16 a[32:47], a[148:151], a[60:63], a[32:47]\n"
        "ds_read_b128 a[76:79], v199 offset:49152\n"
        "v_add_f32 v236, 1.0, v236\n"
        "v_add_f32 v237, 1.0, v237\n"
        "v_rcp_f32 v236, v236\n"
        "v_rcp_f32 v237, v237\n"
        "v_mul_f32 v202, v234, v236\n"
        "s_waitcnt vmcnt(22) lgkmcnt(2)\n"
        "v_mfma_f32_32x32x16_bf16 v[160:175], a[152:155], a[64:67], v[160:175]\n"
        "s_waitcnt lgkmcnt(0)\n"
        "s_barrier\n"
        "v_add_u32 v192, 65536, v192\n"
        "ds_read_b128 a[48:51], v192\n"
        "v_mul_f32 v203, v235, v237\n"
        "v_mfma_f32_32x32x16_bf16 a[32:47], a[152:155], a[68:71], a[32:47]\n"
        "v_add_u32 v196, 65536, v196\n"
        "ds_read_b128 a[52:55], v196\n"
        "v_accvgpr_read_b32 v230, a4\n"
        "v_accvgpr_read_b32 v231, a5\n"
        "v_add_f32 v230, v230, v132\n"
        "v_add_f32 v231, v231, v133\n"
        "s_waitcnt vmcnt(21)\n"
        "v_mfma_f32_32x32x16_bf16 v[160:175], a[156:159], a[72:75], v[160:175]\n"
        "v_add_u32 v193, 65536, v193\n"
        "ds_read_b128 a[56:59], v193\n"
        "v_mul_f32 v232, 0xbfb8aa3b, v230\n"
        "v_mul_f32 v233, 0xbfb8aa3b, v231\n"
        "v_exp_f32 v232, v232\n"
        "v_mfma_f32_32x32x16_bf16 a[32:47], a[156:159], a[76:79], a[32:47]\n"
        "v_add_u32 v197, 65536, v197\n"
        "ds_read_b128 a[60:63], v197\n"
        "v_exp_f32 v233, v233\n"
        "v_add_f32 v232, 1.0, v232\n"
        "v_add_f32 v233, 1.0, v233\n"
        "v_rcp_f32 v232, v232\n"
        "s_waitcnt vmcnt(20) lgkmcnt(2)\n"
        "v_mfma_f32_32x32x16_bf16 v[160:175], a[160:163], a[48:51], v[160:175]\n"
        "v_add_u32 v194, 65536, v194\n"
        "ds_read_b128 a[64:67], v194\n"
        "v_rcp_f32 v233, v233\n"
        "v_mul_f32 v204, v230, v232\n"
        "v_mul_f32 v205, v231, v233\n"
        "v_mfma_f32_32x32x16_bf16 a[32:47], a[160:163], a[52:55], a[32:47]\n"
        "v_add_u32 v198, 65536, v198\n"
        "ds_read_b128 a[68:71], v198\n"
        "v_accvgpr_read_b32 v234, a6\n"
        "v_accvgpr_read_b32 v235, a7\n"
        "v_add_f32 v234, v234, v134\n"
        "v_add_f32 v235, v235, v135\n"
        "s_waitcnt vmcnt(19) lgkmcnt(2)\n"
        "v_mfma_f32_32x32x16_bf16 v[160:175], a[164:167], a[56:59], v[160:175]\n"
        "v_add_u32 v195, 65536, v195\n"
        "ds_read_b128 a[72:75], v195\n"
        "v_mul_f32 v236, 0xbfb8aa3b, v234\n"
        "v_mul_f32 v237, 0xbfb8aa3b, v235\n"
        "v_exp_f32 v236, v236\n"
        "v_mfma_f32_32x32x16_bf16 a[32:47], a[164:167], a[60:63], a[32:47]\n"
        "v_add_u32 v199, 65536, v199\n"
        "ds_read_b128 a[76:79], v199\n"
        "v_exp_f32 v237, v237\n"
        "v_add_f32 v236, 1.0, v236\n"
        "v_add_f32 v237, 1.0, v237\n"
        "v_rcp_f32 v236, v236\n"
        "s_waitcnt vmcnt(18) lgkmcnt(2)\n"
        "v_mfma_f32_32x32x16_bf16 v[160:175], a[168:171], a[64:67], v[160:175]\n"
        "s_waitcnt lgkmcnt(0)\n"
        "s_barrier\n"
        "ds_read_b128 a[48:51], v192 offset:16384\n"
        "v_rcp_f32 v237, v237\n"
        "v_mul_f32 v206, v234, v236\n"
        "v_mfma_f32_32x32x16_bf16 a[32:47], a[168:171], a[68:71], a[32:47]\n"
        "ds_read_b128 a[52:55], v196 offset:16384\n"
        "v_mul_f32 v207, v235, v237\n"
        "v_accvgpr_read_b32 v230, a8\n"
        "v_accvgpr_read_b32 v231, a9\n"
        "v_add_f32 v230, v230, v136\n"
        "v_add_f32 v231, v231, v137\n"
        "s_waitcnt vmcnt(17)\n"
        "v_mfma_f32_32x32x16_bf16 v[160:175], a[172:175], a[72:75], v[160:175]\n"
        "ds_read_b128 a[56:59], v193 offset:16384\n"
        "v_mul_f32 v232, 0xbfb8aa3b, v230\n"
        "v_mul_f32 v233, 0xbfb8aa3b, v231\n"
        "v_exp_f32 v232, v232\n"
        "v_exp_f32 v233, v233\n"
        "v_mfma_f32_32x32x16_bf16 a[32:47], a[172:175], a[76:79], a[32:47]\n"
        "ds_read_b128 a[60:63], v197 offset:16384\n"
        "v_add_f32 v232, 1.0, v232\n"
        "v_add_f32 v233, 1.0, v233\n"
        "v_rcp_f32 v232, v232\n"
        "v_rcp_f32 v233, v233\n"
        "v_mul_f32 v208, v230, v232\n"
        "s_waitcnt vmcnt(16) lgkmcnt(2)\n"
        "v_mfma_f32_32x32x16_bf16 v[160:175], a[176:179], a[48:51], v[160:175]\n"
        "ds_read_b128 a[64:67], v194 offset:16384\n"
        "v_mul_f32 v209, v231, v233\n"
        "v_accvgpr_read_b32 v234, a10\n"
        "v_accvgpr_read_b32 v235, a11\n"
        "v_add_f32 v234, v234, v138\n"
        "v_mfma_f32_32x32x16_bf16 a[32:47], a[176:179], a[52:55], a[32:47]\n"
        "ds_read_b128 a[68:71], v198 offset:16384\n"
        "v_add_f32 v235, v235, v139\n"
        "v_mul_f32 v236, 0xbfb8aa3b, v234\n"
        "v_mul_f32 v237, 0xbfb8aa3b, v235\n"
        "v_exp_f32 v236, v236\n"
        "v_exp_f32 v237, v237\n"
        "s_waitcnt vmcnt(15) lgkmcnt(2)\n"
        "v_mfma_f32_32x32x16_bf16 v[160:175], a[180:183], a[56:59], v[160:175]\n"
        "ds_read_b128 a[72:75], v195 offset:16384\n"
        "v_add_f32 v236, 1.0, v236\n"
        "v_add_f32 v237, 1.0, v237\n"
        "v_rcp_f32 v236, v236\n"
        "v_rcp_f32 v237, v237\n"
        "v_mfma_f32_32x32x16_bf16 a[32:47], a[180:183], a[60:63], a[32:47]\n"
        "ds_read_b128 a[76:79], v199 offset:16384\n"
        "v_mul_f32 v210, v234, v236\n"
        "v_mul_f32 v211, v235, v237\n"
        "v_accvgpr_read_b32 v230, a12\n"
        "v_accvgpr_read_b32 v231, a13\n"
        "v_add_f32 v230, v230, v140\n"
        "s_waitcnt vmcnt(14) lgkmcnt(2)\n"
        "v_mfma_f32_32x32x16_bf16 v[160:175], a[184:187], a[64:67], v[160:175]\n"
        "s_waitcnt lgkmcnt(0)\n"
        "s_barrier\n"
        "ds_read_b128 a[48:51], v192 offset:32768\n"
        "v_add_f32 v231, v231, v141\n"
        "v_mul_f32 v232, 0xbfb8aa3b, v230\n"
        "v_mfma_f32_32x32x16_bf16 a[32:47], a[184:187], a[68:71], a[32:47]\n"
        "ds_read_b128 a[52:55], v196 offset:32768\n"
        "v_mul_f32 v233, 0xbfb8aa3b, v231\n"
        "v_exp_f32 v232, v232\n"
        "v_exp_f32 v233, v233\n"
        "v_add_f32 v232, 1.0, v232\n"
        "v_add_f32 v233, 1.0, v233\n"
        "s_waitcnt vmcnt(13)\n"
        "v_mfma_f32_32x32x16_bf16 v[160:175], a[188:191], a[72:75], v[160:175]\n"
        "ds_read_b128 a[56:59], v193 offset:32768\n"
        "v_rcp_f32 v232, v232\n"
        "v_rcp_f32 v233, v233\n"
        "v_mul_f32 v212, v230, v232\n"
        "v_mul_f32 v213, v231, v233\n"
        "v_mfma_f32_32x32x16_bf16 a[32:47], a[188:191], a[76:79], a[32:47]\n"
        "ds_read_b128 a[60:63], v197 offset:32768\n"
        "v_accvgpr_read_b32 v234, a14\n"
        "v_accvgpr_read_b32 v235, a15\n"
        "v_add_f32 v234, v234, v142\n"
        "v_add_f32 v235, v235, v143\n"
        "v_mul_f32 v236, 0xbfb8aa3b, v234\n"
        "s_waitcnt vmcnt(12) lgkmcnt(2)\n"
        "v_mfma_f32_32x32x16_bf16 v[160:175], a[192:195], a[48:51], v[160:175]\n"
        "ds_read_b128 a[64:67], v194 offset:32768\n"
        "v_mul_f32 v237, 0xbfb8aa3b, v235\n"
        "v_exp_f32 v236, v236\n"
        "v_exp_f32 v237, v237\n"
        "v_add_f32 v236, 1.0, v236\n"
        "v_mfma_f32_32x32x16_bf16 a[32:47], a[192:195], a[52:55], a[32:47]\n"
        "ds_read_b128 a[68:71], v198 offset:32768\n"
        "v_add_f32 v237, 1.0, v237\n"
        "v_rcp_f32 v236, v236\n"
        "v_rcp_f32 v237, v237\n"
        "v_mul_f32 v214, v234, v236\n"
        "v_mul_f32 v215, v235, v237\n"
        "s_waitcnt vmcnt(11) lgkmcnt(2)\n"
        "v_mfma_f32_32x32x16_bf16 v[160:175], a[196:199], a[56:59], v[160:175]\n"
        "ds_read_b128 a[72:75], v195 offset:32768\n"
        "v_mfma_f32_32x32x16_bf16 a[32:47], a[196:199], a[60:63], a[32:47]\n"
        "ds_read_b128 a[76:79], v199 offset:32768\n"
        "s_waitcnt vmcnt(10) lgkmcnt(2)\n"
        "v_mfma_f32_32x32x16_bf16 v[160:175], a[200:203], a[64:67], v[160:175]\n"
        "s_waitcnt lgkmcnt(0)\n"
        "s_barrier\n"
        "ds_read_b128 a[48:51], v192 offset:49152\n"
        "v_mfma_f32_32x32x16_bf16 a[32:47], a[200:203], a[68:71], a[32:47]\n"
        "ds_read_b128 a[52:55], v196 offset:49152\n"
        "s_waitcnt vmcnt(9)\n"
        "v_mfma_f32_32x32x16_bf16 v[160:175], a[204:207], a[72:75], v[160:175]\n"
        "ds_read_b128 a[56:59], v193 offset:49152\n"
        "v_mfma_f32_32x32x16_bf16 a[32:47], a[204:207], a[76:79], a[32:47]\n"
        "ds_read_b128 a[60:63], v197 offset:49152\n"
        "s_waitcnt vmcnt(8) lgkmcnt(2)\n"
        "v_mfma_f32_32x32x16_bf16 v[160:175], a[208:211], a[48:51], v[160:175]\n"
        "ds_read_b128 a[64:67], v194 offset:49152\n"
        "v_mfma_f32_32x32x16_bf16 a[32:47], a[208:211], a[52:55], a[32:47]\n"
        "ds_read_b128 a[68:71], v198 offset:49152\n"
        "s_waitcnt vmcnt(7) lgkmcnt(2)\n"
        "v_mfma_f32_32x32x16_bf16 v[160:175], a[212:215], a[56:59], v[160:175]\n"
        "ds_read_b128 a[72:75], v195 offset:49152\n"
        "v_mfma_f32_32x32x16_bf16 a[32:47], a[212:215], a[60:63], a[32:47]\n"
        "ds_read_b128 a[76:79], v199 offset:49152\n"
        "s_waitcnt vmcnt(6) lgkmcnt(2)\n"
        "v_mfma_f32_32x32x16_bf16 v[160:175], a[216:219], a[64:67], v[160:175]\n"
        "s_waitcnt lgkmcnt(0)\n"
        "s_barrier\n"
        "v_add_u32 v192, 65536, v192\n"
        "ds_read_b128 a[48:51], v192\n"
        "v_mfma_f32_32x32x16_bf16 a[32:47], a[216:219], a[68:71], a[32:47]\n"
        "v_add_u32 v196, 65536, v196\n"
        "ds_read_b128 a[52:55], v196\n"
        "s_waitcnt vmcnt(5)\n"
        "v_mfma_f32_32x32x16_bf16 v[160:175], a[220:223], a[72:75], v[160:175]\n"
        "v_add_u32 v193, 65536, v193\n"
        "ds_read_b128 a[56:59], v193\n"
        "v_mfma_f32_32x32x16_bf16 a[32:47], a[220:223], a[76:79], a[32:47]\n"
        "v_add_u32 v197, 65536, v197\n"
        "ds_read_b128 a[60:63], v197\n"
        "s_waitcnt vmcnt(4) lgkmcnt(2)\n"
        "v_mfma_f32_32x32x16_bf16 v[160:175], a[224:227], a[48:51], v[160:175]\n"
        "v_add_u32 v194, 65536, v194\n"
        "ds_read_b128 a[64:67], v194\n"
        "v_mfma_f32_32x32x16_bf16 a[32:47], a[224:227], a[52:55], a[32:47]\n"
        "v_add_u32 v198, 65536, v198\n"
        "ds_read_b128 a[68:71], v198\n"
        "s_waitcnt vmcnt(3) lgkmcnt(2)\n"
        "v_mfma_f32_32x32x16_bf16 v[160:175], a[228:231], a[56:59], v[160:175]\n"
        "v_add_u32 v195, 65536, v195\n"
        "ds_read_b128 a[72:75], v195\n"
        "v_mfma_f32_32x32x16_bf16 a[32:47], a[228:231], a[60:63], a[32:47]\n"
        "v_add_u32 v199, 65536, v199\n"
        "ds_read_b128 a[76:79], v199\n"
        "s_waitcnt vmcnt(2) lgkmcnt(2)\n"
        "v_mfma_f32_32x32x16_bf16 v[160:175], a[232:235], a[64:67], v[160:175]\n"
        "v_mfma_f32_32x32x16_bf16 a[32:47], a[232:235], a[68:71], a[32:47]\n"
        "s_waitcnt vmcnt(1) lgkmcnt(0)\n"
        "v_mfma_f32_32x32x16_bf16 a[32:47], a[236:239], a[76:79], a[32:47]\n"
        "v_mfma_f32_32x32x16_bf16 v[160:175], a[236:239], a[72:75], v[160:175]\n"
        "v_add_u32 v228, s72, v1\n"
        "s_xor_b32 s95, s72, 8192\n"
        "v_add_u32 v229, s95, v1\n"
        "s_nop 7\n"
        "ds_write_b128 v228, a[32:35] offset:16384\n"
        "ds_write_b128 v228, a[36:39] offset:17408\n"
        "ds_write_b128 v228, a[40:43] offset:18432\n"
        "ds_write_b128 v228, a[44:47] offset:19456\n"
        "v_accvgpr_read_b32 v225, a111\n"
        "v_lshlrev_b32 v225, 1, v225\n"
        "s_waitcnt vmcnt(0)\n"
        "s_mov_b32 s99, 400000\n"
        ".Lp4_%=:\n"
        "v_cmp_eq_u64 vcc, s[96:97], v[220:221]\n"
        "v_cmp_eq_u32 s[100:101], 0, v223\n"
        "s_nop 0\n"
        "s_or_b64 vcc, vcc, s[100:101]\n"
        "s_cmp_eq_u64 vcc, -1\n"
        "s_cbranch_scc1 .Lq4_%=\n"
        "s_sub_u32 s99, s99, 1\n"
        "s_cmp_eq_u32 s99, 0\n"
        "s_cbranch_scc1 .Lq4_%=\n"
        "s_sleep 1\n"
        "buffer_load_dwordx2 v[220:221], v222, s[56:59], 0 offen sc1\n"
        "s_waitcnt vmcnt(0)\n"
        "s_nop 1\n"
        "s_branch .Lp4_%=\n"
        ".Lq4_%=:\n"
        "s_waitcnt lgkmcnt(0)\n"
        "s_barrier\n"
        "ds_read_b128 v[128:131], v229 offset:16384\n"
        "ds_read_b128 v[132:135], v229 offset:17408\n"
        "ds_read_b128 v[136:139], v229 offset:18432\n"
        "ds_read_b128 v[140:143], v229 offset:19456\n"
        "s_cmp_eq_u32 s3, 0\n"
        "s_cselect_b32 s95, 1, 0\n"
        "v_cmp_gt_u32 vcc, s95, v252\n"
        "v_mov_b32 v226, 2147479552\n"
        "v_mov_b32 v227, s96\n"
        "v_cndmask_b32 v226, v226, 0, vcc\n"
        "buffer_store_dword v227, v226, s[56:59], 0 offen sc1\n"
        "s_waitcnt lgkmcnt(3)\n"
        "v_add_f32 v160, v160, v128\n"
        "v_fma_f32 v160, v160, v200, v176\n"
        "buffer_store_dword v160, v225, s[60:63], 0 offen sc1 nt\n"
        "v_add_f32 v161, v161, v129\n"
        "v_fma_f32 v161, v161, v201, v177\n"
        "buffer_store_dword v161, v225, s[60:63], 0 offen offset:1792 sc1 nt\n"
        "v_add_f32 v162, v162, v130\n"
        "v_fma_f32 v162, v162, v202, v178\n"
        "buffer_store_dword v162, v225, s[60:63], s81 offen sc1 nt\n"
        "v_add_f32 v163, v163, v131\n"
        "v_fma_f32 v163, v163, v203, v179\n"
        "buffer_store_dword v163, v225, s[60:63], s81 offen offset:1792 sc1 nt\n"
        "s_waitcnt lgkmcnt(2)\n"
        "v_add_f32 v164, v164, v132\n"
        "v_fma_f32 v164, v164, v204, v180\n"
        "buffer_store_dword v164, v225, s[60:63], s82 offen sc1 nt\n"
        "v_add_f32 v165, v165, v133\n"
        "v_fma_f32 v165, v165, v205, v181\n"
        "buffer_store_dword v165, v225, s[60:63], s82 offen offset:1792 sc1 nt\n"
        "v_add_f32 v166, v166, v134\n"
        "v_fma_f32 v166, v166, v206, v182\n"
        "buffer_store_dword v166, v225, s[60:63], s83 offen sc1 nt\n"
        "v_add_f32 v167, v167, v135\n"
        "v_fma_f32 v167, v167, v207, v183\n"
        "buffer_store_dword v167, v225, s[60:63], s83 offen offset:1792 sc1 nt\n"
        "s_waitcnt lgkmcnt(1)\n"
        "v_add_f32 v168, v168, v136\n"
        "v_fma_f32 v168, v168, v208, v184\n"
        "buffer_store_dword v168, v225, s[60:63], s84 offen sc1 nt\n"
        "v_add_f32 v169, v169, v137\n"
        "v_fma_f32 v169, v169, v209, v185\n"
        "buffer_store_dword v169, v225, s[60:63], s84 offen offset:1792 sc1 nt\n"
        "v_add_f32 v170, v170, v138\n"
        "v_fma_f32 v170, v170, v210, v186\n"
        "buffer_store_dword v170, v225, s[60:63], s85 offen sc1 nt\n"
        "v_add_f32 v171, v171, v139\n"
        "v_fma_f32 v171, v171, v211, v187\n"
        "buffer_store_dword v171, v225, s[60:63], s85 offen offset:1792 sc1 nt\n"
        "s_waitcnt lgkmcnt(0)\n"
        "v_add_f32 v172, v172, v140\n"
        "v_fma_f32 v172, v172, v212, v188\n"
        "buffer_store_dword v172, v225, s[60:63], s86 offen sc1 nt\n"
        "v_add_f32 v173, v173, v141\n"
        "v_fma_f32 v173, v173, v213, v189\n"
        "buffer_store_dword v173, v225, s[60:63], s86 offen offset:1792 sc1 nt\n"
        "v_add_f32 v174, v174, v142\n"
        "v_fma_f32 v174, v174, v214, v190\n"
        "buffer_store_dword v174, v225, s[60:63], s87 offen sc1 nt\n"
        "v_add_f32 v175, v175, v143\n"
        "v_fma_f32 v175, v175, v215, v191\n"
        "buffer_store_dword v175, v225, s[60:63], s87 offen offset:1792 sc1 nt\n"
        ".Lend%=:\n"
        :
        : "s"(__builtin_amdgcn_kernarg_segment_ptr()), "s"(blockIdx.x), "v"(threadIdx.x)
        : "v1", "v2", "v3", "v4", "v5", "v6", "v7", "v8", "v9", "v10", "v11", "v12", "v13", "v14", "v15", "v16", "v17", "v18", "v19", "v20", "v21", "v22", "v23", "v24", "v25", "v26", "v27", "v28", "v29", "v30", "v31", "v32", "v33", "v34", "v35", "v36", "v37", "v38", "v39", "v40", "v41", "v42", "v43", "v44", "v45", "v46", "v47", "v48", "v49", "v50", "v51", "v52", "v53", "v54", "v55", "v56", "v57", "v58", "v59", "v60", "v61", "v62", "v63", "v64", "v65", "v66", "v67", "v68", "v69", "v70", "v71", "v72", "v73", "v74", "v75", "v76", "v77", "v78", "v79", "v80", "v81", "v82", "v83", "v84", "v85", "v86", "v87", "v88", "v89", "v90", "v91", "v92", "v93", "v94", "v95", "v96", "v97", "v98", "v99", "v100", "v101", "v102", "v103", "v104", "v105", "v106", "v107", "v108", "v109", "v110", "v111", "v112", "v113", "v114", "v115", "v116", "v117", "v118", "v119", "v120", "v121", "v122", "v123", "v124", "v125", "v126", "v127", "v128", "v129", "v130", "v131", "v132", "v133", "v134", "v135", "v136", "v137", "v138", "v139", "v140", "v141", "v142", "v143", "v144", "v145", "v146", "v147", "v148", "v149", "v150", "v151", "v152", "v153", "v154", "v155", "v156", "v157", "v158", "v159", "v160", "v161", "v162", "v163", "v164", "v165", "v166", "v167", "v168", "v169", "v170", "v171", "v172", "v173", "v174", "v175", "v176", "v177", "v178", "v179", "v180", "v181", "v182", "v183", "v184", "v185", "v186", "v187", "v188", "v189", "v190", "v191", "v192", "v193", "v194", "v195", "v196", "v197", "v198", "v199", "v200", "v201", "v202", "v203", "v204", "v205", "v206", "v207", "v208", "v209", "v210", "v211", "v212", "v213", "v214", "v215", "v216", "v217", "v218", "v219", "v220", "v221", "v222", "v223", "v224", "v225", "v226", "v227", "v228", "v229", "v230", "v231", "v232", "v233", "v234", "v235", "v236", "v237", "v238", "v239", "v240", "v241", "v242", "v243", "v244", "v245", "v246", "v247", "v248", "v249", "v250", "v251", "v252", "v253", "v254", "v255", "a0", "a1", "a2", "a3", "a4", "a5", "a6", "a7", "a8", "a9", "a10", "a11", "a12", "a13", "a14", "a15", "a16", "a17", "a18", "a19", "a20", "a21", "a22", "a23", "a24", "a25", "a26", "a27", "a28", "a29", "a30", "a31", "a32", "a33", "a34", "a35", "a36", "a37", "a38", "a39", "a40", "a41", "a42", "a43", "a44", "a45", "a46", "a47", "a48", "a49", "a50", "a51", "a52", "a53", "a54", "a55", "a56", "a57", "a58", "a59", "a60", "a61", "a62", "a63", "a64", "a65", "a66", "a67", "a68", "a69", "a70", "a71", "a72", "a73", "a74", "a75", "a76", "a77", "a78", "a79", "a80", "a81", "a82", "a83", "a84", "a85", "a86", "a87", "a88", "a89", "a90", "a91", "a92", "a93", "a94", "a95", "a96", "a97", "a98", "a99", "a100", "a101", "a102", "a103", "a104", "a105", "a106", "a107", "a108", "a109", "a110", "a111", "a112", "a113", "a114", "a115", "a116", "a117", "a118", "a119", "a120", "a121", "a122", "a123", "a124", "a125", "a126", "a127", "a128", "a129", "a130", "a131", "a132", "a133", "a134", "a135", "a136", "a137", "a138", "a139", "a140", "a141", "a142", "a143", "a144", "a145", "a146", "a147", "a148", "a149", "a150", "a151", "a152", "a153", "a154", "a155", "a156", "a157", "a158", "a159", "a160", "a161", "a162", "a163", "a164", "a165", "a166", "a167", "a168", "a169", "a170", "a171", "a172", "a173", "a174", "a175", "a176", "a177", "a178", "a179", "a180", "a181", "a182", "a183", "a184", "a185", "a186", "a187", "a188", "a189", "a190", "a191", "a192", "a193", "a194", "a195", "a196", "a197", "a198", "a199", "a200", "a201", "a202", "a203", "a204", "a205", "a206", "a207", "a208", "a209", "a210", "a211", "a212", "a213", "a214", "a215", "a216", "a217", "a218", "a219", "a220", "a221", "a222", "a223", "a224", "a225", "a226", "a227", "a228", "a229", "a230", "a231", "a232", "a233", "a234", "a235", "a236", "a237", "a238", "a239", "a240", "a241", "a242", "a243", "a244", "a245", "a246", "a247", "a248", "a249", "a250", "a251", "a252", "a253", "a254", "a255", "s3", "s4", "s5", "s6", "s7", "s8", "s9", "s10", "s11", "s12", "s13", "s14", "s15", "s16", "s17", "s18", "s19", "s20", "s21", "s22", "s23", "s24", "s25", "s26", "s27", "s28", "s29", "s30", "s31", "s32", "s33", "s34", "s35", "s36", "s37", "s38", "s39", "s40", "s41", "s42", "s43", "s44", "s45", "s46", "s47", "s48", "s49", "s50", "s51", "s52", "s53", "s54", "s55", "s56", "s57", "s58", "s59", "s60", "s61", "s62", "s63", "s64", "s65", "s66", "s67", "s68", "s69", "s70", "s71", "s72", "s73", "s74", "s75", "s76", "s77", "s78", "s79", "s80", "s81", "s82", "s83", "s84", "s85", "s86", "s87", "s88", "s89", "s90", "s91", "s92", "s93", "s94", "s95", "s96", "s97", "s98", "s99", "s100", "s101", "vcc", "scc", "m0", "memory");
}

extern "C" void kernel_launch(void* const* d_in, const int* in_sizes, int n_in, void* d_out, int out_size, void* d_ws,
                              size_t ws_size, hipStream_t stream) {
    (void)in_sizes; (void)n_in; (void)out_size; (void)ws_size;
    hipFuncSetAttribute((const void*)s5_fused, hipFuncAttributeMaxDynamicSharedMemorySize, 163840);
    s5_fused<<<224, 256, 163840, stream>>>((const uint16_t*)d_in[0], (const uint16_t*)d_in[1], (const uint16_t*)d_in[2], (const uint16_t*)d_in[3],
        (const uint16_t*)d_in[4], (const uint16_t*)d_in[5], (const uint16_t*)d_in[6], (const uint16_t*)d_in[7], (const uint16_t*)d_in[8],
        (const uint16_t*)d_in[10], (const uint16_t*)d_in[11], (const uint16_t*)d_in[12], (float*)d_out, (char*)d_ws);
}
```
